```python
import torch

def _softmax(x, axis=-1):
    return torch.softmax(x, dim=axis)
def _gelu(x):
    return torch.nn.functional.gelu(x, approximate="tanh")
def _silu(x):
    return torch.nn.functional.silu(x)
def _sigmoid(x):
    return torch.sigmoid(x)
def _layernorm(x, axis=-1, eps=1e-5):
    m = x.mean(dim=axis, keepdim=True)
    v = x.var(dim=axis, keepdim=True, unbiased=False)
    return (x - m) / torch.sqrt(v + eps)
def _scatter_add(tgt, idx, src, axis=0):
    return tgt.clone().index_add_(axis, idx, src)
def _segment_sum(src, seg, n):
    out = torch.zeros((n,) + src.shape[1:], dtype=src.dtype)
    return out.index_add_(0, seg, src)
def _pad(x, pw):
    flat = tuple(v for pair in reversed(pw) for v in pair)
    return torch.nn.functional.pad(x, flat)

kernel_name = 'interleave_layers__sliding_local_dilated__glu_gated_swiglu__prompt'

def setup_inputs():

    _w = (lambda a: (lambda t: t.to(torch.bfloat16) if t.is_floating_point() else t)(torch.as_tensor(a)))
    def _A_setup():
        B, S, D = 1, 2048, 448
        return {
            "x": _w((lambda *s: torch.randn(*s, dtype=torch.bfloat16) * 0.05)(B, S, D)),
            "wqkv": _w((lambda *s: torch.randn(*s, dtype=torch.bfloat16) * 0.05)(D, 3 * D)),
            "wo": _w((lambda *s: torch.randn(*s, dtype=torch.bfloat16) * 0.05)(D, D)),
        }
    _A_in = _A_setup()

    def _B_setup():
        B, S, D, F = 1, 2048, 448, 1792
        return {
            "x": _w((lambda *s: torch.randn(*s, dtype=torch.bfloat16) * 0.05)(B, S, D)),
            "w1": _w((lambda *s: torch.randn(*s, dtype=torch.bfloat16) * 0.8)(D, F) * (2.0 / D) ** 0.5),
            "w3": _w((lambda *s: torch.randn(*s, dtype=torch.bfloat16) * 0.8)(D, F) * (2.0 / D) ** 0.5),
            "w2": _w((lambda *s: torch.randn(*s, dtype=torch.bfloat16) * 0.8)(F, D) * (2.0 / F) ** 0.5),
        }
    _B_in = _B_setup()

    return {
        "A_x": _A_in["x"],
        "A_wqkv": _A_in["wqkv"],
        "A_wo": _A_in["wo"],
        "B_x": _B_in["x"],
        "B_w1": _B_in["w1"],
        "B_w3": _B_in["w3"],
        "B_w2": _B_in["w2"],
    }

def reference(A_x, A_wqkv, A_wo, B_x, B_w1, B_w3, B_w2):
    def _A_ref(x, wqkv, wo):
        B, S, D = x.shape
        H = 16; Dh = D // H
        W, dil = 8, 2
        qkv = (lambda x, *d: x.permute(*d))((x @ wqkv).reshape(B, S, 3, H, Dh), 2, 0, 3, 1, 4)
        q, k, v = qkv[0], qkv[1], qkv[2]
        scale = Dh ** -0.5
        pos = torch.arange(S)
        off = torch.arange(W) * dil
        idx = pos[:, None] - off[None, :]
        mask = idx >= 0
        idx_c = torch.where(mask, idx, 0).reshape(-1)
        kg = k[:, :, idx_c, :].reshape(B, H, S, W, Dh)
        vg = v[:, :, idx_c, :].reshape(B, H, S, W, Dh)
        logits = torch.einsum("bhsd,bhswd->bhsw", q, kg) * scale
        logits = torch.where(mask[None, None, :, :], logits, -1e9)
        attn = _softmax(logits, axis=-1)
        o = torch.einsum("bhsw,bhswd->bhsd", attn, vg)
        return (lambda x, *d: x.permute(*d))(o, 0, 2, 1, 3).reshape(B, S, D) @ wo

    def _B_ref(x, w1, w3, w2):
        gate = torch.einsum("bsd,df->bsf", x, w1)
        up = torch.einsum("bsd,df->bsf", x, w3)
        h = _silu(gate) * up
        out = torch.einsum("bsf,fd->bsd", h, w2)
        return out

    x = A_x
    x = x + _A_ref(_layernorm(x, axis=-1), A_wqkv, A_wo)
    x = x + _B_ref(_layernorm(x, axis=-1), B_w1, B_w3, B_w2)
    x = x + _A_ref(_layernorm(x, axis=-1), A_wqkv, A_wo)
    x = x + _B_ref(_layernorm(x, axis=-1), B_w1, B_w3, B_w2)
    return x
```

```cpp
#include <hip/hip_runtime.h>
#include <hip/hip_bf16.h>

typedef __attribute__((ext_vector_type(2))) __bf16 bf2;
typedef __attribute__((ext_vector_type(8))) __bf16 bf8;
typedef __attribute__((ext_vector_type(2))) float f2;
typedef __attribute__((ext_vector_type(4))) float f4;
typedef __attribute__((ext_vector_type(2))) unsigned u2;
typedef __attribute__((ext_vector_type(4))) unsigned u4;
typedef __attribute__((address_space(3))) void* ldsp;
typedef __attribute__((address_space(1))) unsigned gu32;
typedef __amdgpu_buffer_rsrc_t rsrc_t;
#define DI __device__ __forceinline__

constexpr int KB = 1024;
constexpr int O_WQKV = 0, O_WO = O_WQKV + 1344 * KB, O_W13 = O_WO + 448 * KB, O_W2 = O_W13 + 3136 * KB;
constexpr int O_X = O_W2 + 1792 * KB, XSZ = 1792 * KB;
constexpr int O_O = O_X + 4 * XSZ, OSZ = 2048 * KB;
constexpr int O_H = O_O + 2 * OSZ, HSZ = 7168 * KB;
constexpr int O_CNT = O_H + 2 * HSZ, CNT_WORDS = 8 * 32 * 64;
constexpr int O_DBG = O_CNT + CNT_WORDS * 4;
constexpr int WS_BYTES = O_DBG + 256 * 64 * 8;
constexpr int LDS_STAT = 128 * KB, LDS_BYTES = 132 * KB;
constexpr int SC1 = 16;

DI f4 mfma(u4 a, u4 b, f4 c) {
    return __builtin_amdgcn_mfma_f32_16x16x32_bf16(__builtin_bit_cast(bf8, a), __builtin_bit_cast(bf8, b), c, 0, 0, 0);
}
DI float dot2(unsigned a, unsigned b, float c) {
    return __builtin_amdgcn_fdot2_f32_bf16(__builtin_bit_cast(bf2, a), __builtin_bit_cast(bf2, b), c, false);
}
DI unsigned pk(float a, float b) { f2 v = {a, b}; return __builtin_bit_cast(unsigned, __builtin_convertvector(v, bf2)); }
DI float lo(unsigned x) { return __builtin_bit_cast(float, x << 16); }
DI float hi(unsigned x) { return __builtin_bit_cast(float, x & 0xffff0000u); }
DI unsigned fu(float x) { return __builtin_bit_cast(unsigned, x); }
DI float uf(unsigned x) { return __builtin_bit_cast(float, x); }
DI float sum4g(float x) {
    u2 a = __builtin_amdgcn_permlane16_swap(fu(x), fu(x), false, false);
    x = uf(a.x) + uf(a.y);
    u2 b = __builtin_amdgcn_permlane32_swap(fu(x), fu(x), false, false);
    return uf(b.x) + uf(b.y);
}
#define WAITV(n) asm volatile("s_waitcnt vmcnt(" #n ")" ::: "memory")
#define BAR() do { asm volatile("s_waitcnt lgkmcnt(0)" ::: "memory"); __builtin_amdgcn_s_barrier(); asm volatile("" ::: "memory"); } while (0)
#define STAMP(i)

__global__ __launch_bounds__(256) void pack_kernel(const unsigned short* x, const unsigned short* wqkv, const unsigned short* wo,
                                                   const unsigned short* w1, const unsigned short* w3, const unsigned short* w2, char* ws) {
    const int lane = threadIdx.x & 63, t = lane & 15, g = lane >> 4;
    const int gw = __builtin_amdgcn_readfirstlane(blockIdx.x * 4 + (threadIdx.x >> 6));
    if (blockIdx.x < CNT_WORDS / 256) ((unsigned*)(ws + O_CNT))[blockIdx.x * 256 + threadIdx.x] = 0;
    u4 xv; const bool hasx = gw < 1792;
    if (hasx) { int m = gw / 14, s = gw % 14; xv = *(const u4*)(x + (m * 16 + t) * 448 + s * 32 + g * 8); }
    unsigned short v[3][8]; int dst[3];
#pragma unroll
    for (int it = 0; it < 3; ++it) {
        int id = gw + it * 2240;
        const unsigned short* src; int ld, col0, ncol = 16, row0, nrow = 32;
        if (id < 3136) { int s = id / 224, r = id % 224, mat = r / 112, nt = r % 112;
            src = mat ? w3 : w1; ld = 1792; col0 = 16 * nt; row0 = 32 * s; dst[it] = O_W13 + ((nt * 2 + mat) * 14 + s) * KB;
        } else if (id < 3136 + 1792) { id -= 3136; int s = id >> 5, c = (id & 31) >> 2, n = id & 3;
            src = w2; ld = 448; col0 = 56 * c + 16 * n; ncol = n == 3 ? 8 : 16; row0 = 32 * s; dst[it] = O_W2 + ((c * 56 + s) * 4 + n) * KB;
        } else if (id < 3136 + 1792 + 1344) { id -= 3136 + 1792; int s = id / 96, nt = id % 96, p = nt >> 5, j = (nt & 31) >> 1, h = nt & 1;
            src = wqkv; ld = 1344; col0 = p * 448 + 28 * j + 16 * h; ncol = h ? 12 : 16; row0 = 32 * s; dst[it] = O_WQKV + ((j * 6 + 2 * p + h) * 14 + s) * KB;
        } else { id -= 3136 + 1792 + 1344; int s = id / 28, nt = id % 28;
            src = wo; ld = 448; col0 = 16 * nt; row0 = 28 * s; nrow = 28; dst[it] = O_WO + (nt * 16 + s) * KB; }
        const rsrc_t r = __builtin_amdgcn_make_buffer_rsrc((void*)src, 0, src == w2 ? 1792 * 448 * 2 : ld * 448 * 2, 0x00020000);
#pragma unroll
        for (int j = 0; j < 8; ++j) { int k = 8 * g + j; bool ok = t < ncol && k < nrow;
            v[it][j] = __builtin_amdgcn_raw_buffer_load_b16(r, ok ? ((row0 + k) * ld + col0 + t) * 2 : 0x7ffffff0, 0, 0); }
    }
    if (hasx) *(u4*)(ws + O_X + gw * KB + lane * 16) = xv;
#pragma unroll
    for (int it = 0; it < 3; ++it) {
        u4 o; o.x = v[it][0] | (unsigned)v[it][1] << 16; o.y = v[it][2] | (unsigned)v[it][3] << 16;
        o.z = v[it][4] | (unsigned)v[it][5] << 16; o.w = v[it][6] | (unsigned)v[it][7] << 16;
        *(u4*)(ws + dst[it] + lane * 16) = o;
    }
}

DI void wait_cnt(gu32* p, unsigned want) {
#pragma unroll 1
    for (int i = 0; i < (1 << 21); ++i) {
        if (__hip_atomic_load(p, __ATOMIC_RELAXED, __HIP_MEMORY_SCOPE_AGENT) >= want) break;
        __builtin_amdgcn_s_sleep(1);
    }
}
DI void signal(gu32* p) { __hip_atomic_fetch_add(p, 1u, __ATOMIC_RELAXED, __HIP_MEMORY_SCOPE_AGENT); }
template <int N> DI void loadW(u4 (&W)[28], rsrc_t rs, int v16, int base) {
#pragma unroll
    for (int i = 0; i < N; ++i) W[i] = __builtin_amdgcn_raw_buffer_load_b128(rs, v16 + (i & 3) * KB, base + (i & ~3) * KB, 0);
}
template <int N> DI void touchW(u4 (&W)[28]) {
#pragma unroll
    for (int i = 0; i < N; ++i) asm volatile("" : "+v"(W[i]));
}
DI void dma(rsrc_t rs, char* lds, int ldsoff, int v16, int goff) {
    __builtin_amdgcn_raw_ptr_buffer_load_lds(rs, (ldsp)(lds + ldsoff), 16, v16, goff, 0, SC1);
}
DI void colsum(const u4 (&W)[28], f4 (&cs)[2]) {
    const u4 ones = {0x3f803f80u, 0x3f803f80u, 0x3f803f80u, 0x3f803f80u};
    cs[0] = f4{0, 0, 0, 0}; cs[1] = f4{0, 0, 0, 0};
#pragma unroll
    for (int s = 0; s < 14; ++s) { cs[0] = mfma(W[s], ones, cs[0]); cs[1] = mfma(W[14 + s], ones, cs[1]); }
}
DI u4 resid_pack(f4 a0, f4 a1, u4 r) {
    float v[8];
#pragma unroll
    for (int i = 0; i < 4; ++i) { u2 s = __builtin_amdgcn_permlane16_swap(fu(a0[i]), fu(a1[i]), false, false); v[i] = uf(s.x); v[4 + i] = uf(s.y); }
    u4 o;
#pragma unroll
    for (int i = 0; i < 4; ++i) { unsigned y = pk(v[2 * i], v[2 * i + 1]); o[i] = pk(lo(r[i]) + lo(y), hi(r[i]) + hi(y)); }
    return o;
}
template <int MODE>
DI void qkv_gemm(const u4 (&W)[28], f4 (&acc)[4][2], char* lds, int v16, int m0, int t, int g) {
    constexpr int NM = MODE == 0 ? 4 : 3;
    float st[3] = {0, 0, 0};
    const char* ap = lds + m0 * KB + v16;
    u4 b[2][NM];
#pragma unroll
    for (int s = 0; s < 14; ++s) {
        const bool first = s == 0 || s == 3 || s == 7 || s == 10, last = s == 2 || s == 6 || s == 9 || s == 13;
        if (s == 0) { WAITV(12); BAR(); } else if (s == 3) { WAITV(8); BAR(); } else if (s == 7) { WAITV(4); BAR(); } else if (s == 10) { WAITV(0); BAR(); }
#pragma unroll
        for (int mi = 0; mi < NM; ++mi) {
            if (first) b[s & 1][mi] = *(const u4*)(ap + (s * 9 + mi) * KB);
        }
#pragma unroll
        for (int mi = 0; mi < NM; ++mi) {
            if (!last) b[(s & 1) ^ 1][mi] = *(const u4*)(ap + (s * 9 + 9 + mi) * KB);
            u4 x = b[s & 1][mi];
            acc[mi][0] = mfma(W[s], x, acc[mi][0]); acc[mi][1] = mfma(W[14 + s], x, acc[mi][1]);
            if (MODE) {
#pragma unroll
                for (int i = 0; i < 4; ++i) st[mi] = dot2(x[i], MODE == 1 ? 0x3f803f80u : x[i], st[mi]);
                asm volatile("" : "+v"(st[mi]));
            }
        }
        __builtin_amdgcn_sched_barrier(0);
    }
    if (MODE) {
#pragma unroll
        for (int mi = 0; mi < 3; ++mi) { float r = sum4g(st[mi]); if (g == 0) *(float*)(lds + LDS_STAT + (((m0 + mi) * 16 + t) * 2 + MODE - 1) * 4) = r; }
    }
}
DI f2 ln_stat(const char* lds, int tok) {
    f2 s = *(const f2*)(lds + LDS_STAT + tok * 8);
    float mean = s.x * (1.0f / 448), var = fmaxf(s.y * (1.0f / 448) - mean * mean, 0.0f);
    return f2{mean, __builtin_amdgcn_rsqf(var + 1e-5f)};
}

__global__ __launch_bounds__(512, 2) void main_kernel(char* ws, char* out) {
    __shared__ __attribute__((aligned(1024))) char lds[LDS_BYTES];
    const rsrc_t rs = __builtin_amdgcn_make_buffer_rsrc(ws, 0, WS_BYTES, 0x00020000);
    gu32* cnt = (gu32*)(ws + O_CNT);
    const int w_ = __builtin_amdgcn_readfirstlane(threadIdx.x >> 6);
#define IDX() int tid = threadIdx.x, w = w_, bid = blockIdx.x; asm volatile("" : "+v"(tid)); asm volatile("" : "+s"(w), "+s"(bid)); \
    const int lane = tid & 63, t = lane & 15, g = lane >> 4, v16 = lane * 16, xc = bid & 7, sl = bid >> 3, w7 = w < 7 ? w : 6; \
    const int T = 4 * (xc >> 1) + (sl >> 3), j = 8 * (xc & 1) + (sl & 7);     \
    const int c2 = 2 * (xc & 1) + (sl & 1), sub2 = (sl >> 1) & 3;             \
    const int c4 = 4 * (xc & 1) + (sl & 3), half4 = (sl >> 2) & 1;            \
    const int p1 = w < 2 ? 0 : w < 5 ? 1 : 2, wq_off = O_WQKV + (j * 6 + 2 * p1) * 14 * KB; \
    (void)t; (void)g; (void)w7; (void)c2; (void)sub2; (void)c4; (void)half4; (void)wq_off; (void)T
    u4 W[28];
    { IDX(); loadW<28>(W, rs, v16, wq_off); }
#pragma unroll 1
    for (int L = 0;; ++L) {
        const int xin = O_X + 2 * L * XSZ, xmid = xin + XSZ, xout = xmid + XSZ, ob = O_O + L * OSZ, hb = O_H + L * HSZ;
        gu32* cn = cnt + L * 4 * 32 * 64;
        {
            IDX();
            STAMP(0);
            const int m0 = w < 2 ? 1 + 4 * w : w < 5 ? 3 * (w - 2) : 3 * (w - 5);
            f4 cs[2]; colsum(W, cs);
            if (L > 0 && w == 0) {
                int i = 2 * T - 1 + lane;
                if (lane < 3 && i >= 0) wait_cnt(cn - 32 * 64 + i * 64, 8);
            }
            BAR();
            STAMP(1);
#pragma unroll
            for (int n = 0; n < 16; ++n) {
                int i = min(w + 8 * n, 125), s = i / 9, m = i - 9 * s, mt = max(8 * T - 1 + m, 0);
                dma(rs, lds, i * KB, v16, xin + (mt * 14 + s) * KB);
            }
            f4 acc[4][2] = {};
            if (p1 == 0) qkv_gemm<0>(W, acc, lds, v16, m0, t, g);
            else if (p1 == 1) qkv_gemm<1>(W, acc, lds, v16, m0, t, g);
            else qkv_gemm<2>(W, acc, lds, v16, m0, t, g);
            BAR();
            STAMP(2);
#pragma unroll
            for (int mi = 0; mi < 4; ++mi) {
                if (mi < 3 || p1 == 0) {
                    int tok = (m0 + mi) * 16 + t;
                    f2 st = ln_stat(lds, tok);
#pragma unroll
                    for (int h = 0; h < 2; ++h) {
                        f4 y = (acc[mi][h] - cs[h] * st.x) * st.y;
                        *(u2*)(lds + ((p1 * 4 + 2 * h + (g >> 1)) * 144 + tok) * 16 + 8 * (g & 1)) = u2{pk(y[0], y[1]), pk(y[2], y[3])};
                    }
                }
            }
            BAR();
            const int tl = 16 + 16 * w + t;
            const char* qp = lds + (g * 144 + tl) * 16;
            u4 q = *(const u4*)qp;
            float lg[8], mx = -3.0e38f;
#pragma unroll
            for (int wi = 0; wi < 8; ++wi) {
                u4 k = *(const u4*)(qp + (4 * 144 - 2 * wi) * 16);
                float d = dot2(q.x, k.x, 0.0f); d = dot2(q.y, k.y, d); d = dot2(q.z, k.z, d); d = dot2(q.w, k.w, d);
                d = sum4g(d) * 0.18898223650461361f;
                lg[wi] = (128 * T + tl - 16 - 2 * wi >= 0) ? d : -1e9f;
                mx = fmaxf(mx, lg[wi]);
            }
            float den = 0, o[8] = {};
#pragma unroll
            for (int wi = 0; wi < 8; ++wi) {
                float p = __builtin_amdgcn_exp2f((lg[wi] - mx) * 1.4426950408889634f);
                den += p;
                u4 v = *(const u4*)(qp + (8 * 144 - 2 * wi) * 16);
#pragma unroll
                for (int i = 0; i < 4; ++i) { o[2 * i] += p * lo(v[i]); o[2 * i + 1] += p * hi(v[i]); }
            }
            float inv = __builtin_amdgcn_rcpf(den);
            u4 ov = {pk(o[0] * inv, o[1] * inv), pk(o[2] * inv, o[3] * inv), pk(o[4] * inv, o[5] * inv), pk(o[6] * inv, o[7] * inv)};
            __builtin_amdgcn_raw_buffer_store_b128(ov, rs, v16, ob + ((8 * T + w) * 16 + j) * KB, SC1);
            STAMP(3);
            WAITV(0);
            loadW<16>(W, rs, v16, O_WO + (7 * c2 + w7) * 16 * KB);
            BAR();
            if (tid == 0) signal(cn + T * 64);
        }
        {
            IDX();
            STAMP(4);
            touchW<16>(W);
            if (tid == 448) wait_cnt(cn + T * 64, 16);
            BAR();
            STAMP(5);
            const int mt0 = 8 * T + 2 * sub2;
#pragma unroll
            for (int n = 0; n < 4; ++n) dma(rs, lds, (w * 4 + n) * KB, v16, ob + (mt0 * 16 + w * 4 + n) * KB);
            const int f = 112 * c2 + 16 * w7 + 8 * (g >> 1);
            const int xo = ((mt0 + (g & 1)) * 14 + (f >> 5)) * KB + (((f & 31) >> 3) * 16 + t) * 16;
            u4 r = __builtin_amdgcn_raw_buffer_load_b128(rs, xo, xin, SC1);
            WAITV(0);
            BAR();
            STAMP(6);
            if (w < 7) {
                f4 a0 = {}, a1 = {};
#pragma unroll
                for (int s = 0; s < 16; ++s) {
                    a0 = mfma(W[s], *(const u4*)(lds + s * KB + v16), a0);
                    a1 = mfma(W[s], *(const u4*)(lds + (16 + s) * KB + v16), a1);
                }
                __builtin_amdgcn_raw_buffer_store_b128(resid_pack(a0, a1, r), rs, xo, xmid, SC1);
            }
            STAMP(7);
            WAITV(0);
            loadW<28>(W, rs, v16, O_W13 + (7 * j + w7) * 28 * KB);
            BAR();
            if (tid == 0) signal(cn + (32 + T) * 64);
        }
        {
            IDX();
            STAMP(8);
            f4 cs[2]; colsum(W, cs);
            if (tid == 448) wait_cnt(cn + (32 + T) * 64, 16);
            BAR();
            STAMP(9);
#pragma unroll
            for (int n = 0; n < 14; ++n) dma(rs, lds, (w + 8 * n) * KB, v16, xmid + (8 * T * 14 + w + 8 * n) * KB);
            const int f = 112 * j + 16 * w7 + 8 * (g >> 1);
            const int ho = ((8 * T + (g & 1)) * 56 + (f >> 5)) * KB + (((f & 31) >> 3) * 16 + t) * 16;
#define P3WAIT(c) do { if (c == 0) WAITV(10); else if (c == 1) WAITV(7); else if (c == 2) WAITV(3); else if (c == 3) WAITV(0); BAR(); } while (0)
            if (w < 7) {
                f4 a[2][2][2];
#pragma unroll
                for (int c = 0; c < 5; ++c) {
                    P3WAIT(c);
                    const char* ap = lds + 2 * c * 14 * KB + v16;
                    u4 b[2][2];
                    f2 st[2];
                    unsigned hp[2][2];
                    if (c < 4) { b[0][0] = *(const u4*)ap; b[0][1] = *(const u4*)(ap + 14 * KB); }
                    if (c > 0) { st[0] = ln_stat(lds, (2 * c - 2) * 16 + t); st[1] = ln_stat(lds, (2 * c - 1) * 16 + t); }
#pragma unroll
                    for (int s = 0; s < 14; ++s) {
                        if (c < 4) {
#pragma unroll
                            for (int mi = 0; mi < 2; ++mi) {
                                if (s < 13) b[(s & 1) ^ 1][mi] = *(const u4*)(ap + (mi * 14 + s + 1) * KB);
                                a[c & 1][mi][0] = mfma(W[s], b[s & 1][mi], s ? a[c & 1][mi][0] : f4{0, 0, 0, 0});
                                a[c & 1][mi][1] = mfma(W[14 + s], b[s & 1][mi], s ? a[c & 1][mi][1] : f4{0, 0, 0, 0});
                            }
                        }
                        if (c > 0 && s >= 1 && s <= 4) {
                            const int mi = (s - 1) >> 1, i0 = ((s - 1) & 1) * 2;
                            float h[2];
#pragma unroll
                            for (int i = 0; i < 2; ++i) {
                                float ga = (a[(c & 1) ^ 1][mi][0][i0 + i] - cs[0][i0 + i] * st[mi].x) * st[mi].y;
                                float up = (a[(c & 1) ^ 1][mi][1][i0 + i] - cs[1][i0 + i] * st[mi].x) * st[mi].y;
                                h[i] = ga * __builtin_amdgcn_rcpf(1.0f + __builtin_amdgcn_exp2f(ga * -1.4426950408889634f)) * up;
                            }
                            hp[mi][i0 >> 1] = pk(h[0], h[1]);
                            asm volatile("" : "+v"(hp[mi][i0 >> 1]));
                        }
                        if (c > 0 && s == 5) {
                            u2 s0 = __builtin_amdgcn_permlane16_swap(hp[0][0], hp[1][0], false, false);
                            u2 s1 = __builtin_amdgcn_permlane16_swap(hp[0][1], hp[1][1], false, false);
                            __builtin_amdgcn_raw_buffer_store_b128(u4{s0.x, s1.x, s0.y, s1.y}, rs, ho, hb + 2 * (c - 1) * 56 * KB, SC1);
                        }
                        if (c < 4) __builtin_amdgcn_sched_barrier(0);
                    }
                    if (c == 3) STAMP(10);
                }
            } else {
#pragma unroll
                for (int c = 0; c < 5; ++c) {
                    P3WAIT(c);
                    if (c < 4) {
                        float s1[2] = {0, 0}, s2[2] = {0, 0};
#pragma unroll
                        for (int s = 0; s < 14; ++s)
#pragma unroll
                            for (int mi = 0; mi < 2; ++mi) {
                                u4 b = *(const u4*)(lds + ((2 * c + mi) * 14 + s) * KB + v16);
#pragma unroll
                                for (int i = 0; i < 4; ++i) { s1[mi] = dot2(b[i], 0x3f803f80u, s1[mi]); s2[mi] = dot2(b[i], b[i], s2[mi]); }
                            }
#pragma unroll
                        for (int mi = 0; mi < 2; ++mi) {
                            f2 r = {sum4g(s1[mi]), sum4g(s2[mi])};
                            if (g == 0) *(f2*)(lds + LDS_STAT + ((2 * c + mi) * 16 + t) * 8) = r;
                        }
                    }
                }
            }
            STAMP(11);
            WAITV(0);
            loadW<28>(W, rs, v16, O_W2 + (c4 * 56 + 7 * w) * 4 * KB);
            BAR();
            if (tid == 0) signal(cn + (64 + T) * 64);
        }
        {
            IDX();
            STAMP(12);
            touchW<28>(W);
            if (tid == 0) wait_cnt(cn + (64 + T) * 64, 16);
            BAR();
            STAMP(13);
            const int mt0 = 8 * T + 4 * half4;
            const int hsrc = hb + (mt0 * 56 + 7 * w) * KB;
            u4 A[3][4];
            f4 acc[4][4] = {};
#pragma unroll
            for (int s = 0; s < 3; ++s)
#pragma unroll
                for (int m = 0; m < 4; ++m) A[s][m] = __builtin_amdgcn_raw_buffer_load_b128(rs, v16, hsrc + (m * 56 + s) * KB, SC1);
#pragma unroll
            for (int s = 0; s < 7; ++s) {
#pragma unroll
                for (int m = 0; m < 4; ++m) {
#pragma unroll
                    for (int n = 0; n < 4; ++n) acc[m][n] = mfma(W[s * 4 + n], A[s % 3][m], acc[m][n]);
                    if (s + 3 < 7) A[s % 3][m] = __builtin_amdgcn_raw_buffer_load_b128(rs, v16, hsrc + (m * 56 + s + 3) * KB, SC1);
                }
                __builtin_amdgcn_sched_barrier(0);
            }
            STAMP(14);
            const int n4 = w & 3, mp = 2 * (w >> 2), f = 56 * c4 + 16 * n4 + 8 * (g >> 1), mt = mt0 + mp + (g & 1);
            const bool ok = n4 < 3 || g < 2;
            const int xo = ok ? (mt * 14 + (f >> 5)) * KB + (((f & 31) >> 3) * 16 + t) * 16 : 0;
            u4 r = __builtin_amdgcn_raw_buffer_load_b128(rs, xo, xmid, SC1);
#pragma unroll
            for (int m = 0; m < 4; ++m)
#pragma unroll
                for (int n = 0; n < 4; ++n) *(f4*)(lds + (w * 16 + m * 4 + n) * KB + v16) = acc[m][n];
            BAR();
            f4 a0 = {}, a1 = {};
#pragma unroll
            for (int s = 0; s < 8; ++s) {
                a0 += *(const f4*)(lds + (s * 16 + mp * 4 + n4) * KB + v16);
                a1 += *(const f4*)(lds + (s * 16 + mp * 4 + 4 + n4) * KB + v16);
            }
            u4 y = resid_pack(a0, a1, r);
            STAMP(15);
            if (L == 1) {
                const rsrc_t ro = __builtin_amdgcn_make_buffer_rsrc(out, 0, 2048 * 448 * 2, 0x00020000);
                if (ok) __builtin_amdgcn_raw_buffer_store_b128(y, ro, ((mt * 16 + t) * 448 + f) * 2, 0, 0);
                STAMP(16);
                break;
            }
            if (ok) __builtin_amdgcn_raw_buffer_store_b128(y, rs, xo, xout, SC1);
            WAITV(0);
            loadW<28>(W, rs, v16, wq_off);
            BAR();
            if (tid == 0) signal(cn + (96 + 2 * T + half4) * 64);
            STAMP(16);
        }
    }
}

extern "C" void kernel_launch(void* const* d_in, const int* in_sizes, int n_in, void* d_out, int out_size, void* d_ws, size_t ws_size,
                              hipStream_t stream) {
    typedef const unsigned short* cp;
    pack_kernel<<<560, 256, 0, stream>>>((cp)d_in[0], (cp)d_in[1], (cp)d_in[2], (cp)d_in[4], (cp)d_in[5], (cp)d_in[6], (char*)d_ws);
    main_kernel<<<256, 512, 0, stream>>>((char*)d_ws, (char*)d_out);
}
```

```cpp
#include <hip/hip_runtime.h>
#include <hip/hip_bf16.h>

typedef __attribute__((ext_vector_type(2))) __bf16 bf2;
typedef __attribute__((ext_vector_type(8))) __bf16 bf8;
typedef __attribute__((ext_vector_type(2))) float f2;
typedef __attribute__((ext_vector_type(4))) float f4;
typedef __attribute__((ext_vector_type(2))) unsigned u2;
typedef __attribute__((ext_vector_type(4))) unsigned u4;
typedef __attribute__((address_space(3))) void* ldsp;
typedef __attribute__((address_space(1))) unsigned gu32;
typedef __amdgpu_buffer_rsrc_t rsrc_t;
#define DI __device__ __forceinline__

constexpr int KB = 1024;
constexpr int O_WQKV = 0, O_WO = O_WQKV + 1344 * KB, O_W13 = O_WO + 448 * KB, O_W2 = O_W13 + 3136 * KB;
constexpr int O_X = O_W2 + 1792 * KB, XSZ = 1792 * KB;
constexpr int O_O = O_X + 4 * XSZ, OSZ = 2048 * KB;
constexpr int O_H = O_O + 2 * OSZ, HSZ = 7168 * KB;
constexpr int O_CNT = O_H + 2 * HSZ, CNT_WORDS = 8 * 32 * 64;
constexpr int O_DBG = O_CNT + CNT_WORDS * 4;
constexpr int WS_BYTES = O_DBG + 256 * 72 * 8;
constexpr int LDS_STAT = 128 * KB, LDS_BYTES = 132 * KB;
constexpr int SC1 = 16;

DI f4 mfma(u4 a, u4 b, f4 c) {
    return __builtin_amdgcn_mfma_f32_16x16x32_bf16(__builtin_bit_cast(bf8, a), __builtin_bit_cast(bf8, b), c, 0, 0, 0);
}
DI float dot2(unsigned a, unsigned b, float c) {
    return __builtin_amdgcn_fdot2_f32_bf16(__builtin_bit_cast(bf2, a), __builtin_bit_cast(bf2, b), c, false);
}
DI unsigned pk(float a, float b) { f2 v = {a, b}; return __builtin_bit_cast(unsigned, __builtin_convertvector(v, bf2)); }
DI float lo(unsigned x) { return __builtin_bit_cast(float, x << 16); }
DI float hi(unsigned x) { return __builtin_bit_cast(float, x & 0xffff0000u); }
DI unsigned fu(float x) { return __builtin_bit_cast(unsigned, x); }
DI float uf(unsigned x) { return __builtin_bit_cast(float, x); }
DI float sum4g(float x) {
    u2 a = __builtin_amdgcn_permlane16_swap(fu(x), fu(x), false, false);
    x = uf(a.x) + uf(a.y);
    u2 b = __builtin_amdgcn_permlane32_swap(fu(x), fu(x), false, false);
    return uf(b.x) + uf(b.y);
}
#define WAITV(n) asm volatile("s_waitcnt vmcnt(" #n ")" ::: "memory")
#define BAR() do { asm volatile("s_waitcnt lgkmcnt(0)" ::: "memory"); __builtin_amdgcn_s_barrier(); asm volatile("" ::: "memory"); } while (0)
#define STAMP(i)

__global__ __launch_bounds__(256) void pack_kernel(const unsigned short* x, const unsigned short* wqkv, const unsigned short* wo,
                                                   const unsigned short* w1, const unsigned short* w3, const unsigned short* w2, char* ws) {
    const int lane = threadIdx.x & 63, t = lane & 15, g = lane >> 4, q = t >> 2, c0 = 4 * (t & 3);
    int id = __builtin_amdgcn_readfirstlane(blockIdx.x * 4 + (threadIdx.x >> 6));
    if (blockIdx.x < CNT_WORDS / 256) ((unsigned*)(ws + O_CNT))[blockIdx.x * 256 + threadIdx.x] = 0;
    if (id >= 1680) {
        id = (id - 1680) * 4;
        u4 v[4];
#pragma unroll
        for (int i = 0; i < 4; ++i) { int m = (id + i) / 14, s = (id + i) % 14; v[i] = *(const u4*)(x + (m * 16 + t) * 448 + s * 32 + g * 8); }
#pragma unroll
        for (int i = 0; i < 4; ++i) *(u4*)(ws + O_X + (id + i) * KB + lane * 16) = v[i];
        return;
    }
    const unsigned short* src; int ld, col, row0, dst, rows = 448; bool ok = true, perm = false, ones = false;
    if (id < 336) { int s = id / 24, nt = (id % 24) * 4 + q, p = nt >> 5, j = (nt & 31) >> 1, h = nt & 1;
        src = wqkv; ld = 1344; col = p * 448 + 28 * j + 16 * h + c0; ok = !(h && c0 == 12); ones = p == 1 && !ok; row0 = 32 * s;
        dst = O_WQKV + ((j * 6 + 2 * p + h) * 14 + s) * KB;
    } else if (id < 336 + 112) { id -= 336; int s = id / 7, nt = (id % 7) * 4 + q;
        src = wo; ld = 448; col = 16 * nt + c0; row0 = 28 * s; perm = true; dst = O_WO + (nt * 16 + s) * KB;
    } else if (id < 448 + 784) { id -= 448; int s = id / 56, r = id % 56, mat = r / 28, nt = (r % 28) * 4 + q;
        src = mat ? w3 : w1; ld = 1792; col = 16 * nt + c0; row0 = 32 * s; dst = O_W13 + ((nt * 2 + mat) * 14 + s) * KB;
    } else { id -= 448 + 784; int s = id >> 3, c = id & 7;
        src = w2; ld = 448; rows = 1792; col = 56 * c + 16 * q + c0; ok = 16 * q + c0 < 56; row0 = 32 * s; dst = O_W2 + ((c * 56 + s) * 4 + q) * KB; }
    const rsrc_t r = __builtin_amdgcn_make_buffer_rsrc((void*)src, 0, rows * ld * 2, 0x00020000);
    u2 v[8];
#pragma unroll
    for (int j = 0; j < 8; ++j) {
        int k = perm ? (j < 4 ? 4 * g + j : 12 + 4 * g + j) : 8 * g + j;
        v[j] = __builtin_amdgcn_raw_buffer_load_b64(r, ok && (!perm || k < 28) ? ((row0 + k) * ld + col) * 2 : 0x7ffffff0, 0, 0);
    }
#pragma unroll
    for (int c = 0; c < 4; ++c) {
        u4 o;
#pragma unroll
        for (int i = 0; i < 4; ++i) o[i] = __builtin_amdgcn_perm(v[2 * i + 1][c >> 1], v[2 * i][c >> 1], (c & 1) ? 0x07060302u : 0x05040100u);
        if (c == 0 && ones) o = u4{0x3f803f80u, 0x3f803f80u, 0x3f803f80u, 0x3f803f80u};
        *(u4*)(ws + dst + (g * 16 + c0 + c) * 16) = o;
    }
}

DI void wait_cnt(gu32* p, unsigned want) {
#pragma unroll 1
    for (int i = 0; i < (1 << 21); ++i) {
        if (__hip_atomic_load(p, __ATOMIC_RELAXED, __HIP_MEMORY_SCOPE_AGENT) >= want) break;
        __builtin_amdgcn_s_sleep(1);
    }
}
DI void signal(gu32* p) { __hip_atomic_fetch_add(p, 1u, __ATOMIC_RELAXED, __HIP_MEMORY_SCOPE_AGENT); }
template <int N, int I0 = 0, int M> DI void loadW(u4 (&W)[M], rsrc_t rs, int v16, int base) {
#pragma unroll
    for (int i = I0; i < N; ++i) W[i] = __builtin_amdgcn_raw_buffer_load_b128(rs, v16 + (i & 3) * KB, base + (i & ~3) * KB, 0);
}
template <int N, int I0 = 0, int M> DI void touchW(u4 (&W)[M]) {
#pragma unroll
    for (int i = I0; i < N; ++i) asm volatile("" : "+v"(W[i]));
}
template <int N, int I0 = 0, int M> DI void undefW(u4 (&W)[M]) {
#pragma unroll
    for (int i = I0; i < N; ++i) asm volatile("" : "=v"(W[i]));
}
DI int opq(int x) { asm volatile("" : "+s"(x)); return x; }
DI void dma(rsrc_t rs, char* lds, int ldsoff, int v16, int goff) {
    __builtin_amdgcn_raw_ptr_buffer_load_lds(rs, (ldsp)(lds + ldsoff), 16, v16, goff, 0, SC1);
}
#define ONES u4{0x3f803f80u, 0x3f803f80u, 0x3f803f80u, 0x3f803f80u}
DI float diag(f4 d, int t) { float a = (t & 1) ? d[1] : d[0], b = (t & 1) ? d[3] : d[2]; return (t & 2) ? b : a; }
template <bool SWAP> DI void colsum(const u4 (&W)[28], f4 (&cs)[2]) {
    const u4 ones = {0x3f803f80u, 0x3f803f80u, 0x3f803f80u, 0x3f803f80u};
    cs[0] = f4{0, 0, 0, 0}; cs[1] = f4{0, 0, 0, 0};
#pragma unroll
    for (int s = 0; s < 14; ++s) {
        cs[0] = SWAP ? mfma(ones, W[s], cs[0]) : mfma(W[s], ones, cs[0]); cs[1] = SWAP ? mfma(ones, W[14 + s], cs[1]) : mfma(W[14 + s], ones, cs[1]);
    }
}
DI u4 resid_pack(f4 a0, f4 a1, u4 r) {
    float v[8];
#pragma unroll
    for (int i = 0; i < 4; ++i) { u2 s = __builtin_amdgcn_permlane16_swap(fu(a0[i]), fu(a1[i]), false, false); v[i] = uf(s.x); v[4 + i] = uf(s.y); }
    u4 o;
#pragma unroll
    for (int i = 0; i < 4; ++i) { unsigned y = pk(v[2 * i], v[2 * i + 1]); o[i] = pk(lo(r[i]) + lo(y), hi(r[i]) + hi(y)); }
    return o;
}
DI f2 ln_fin(f2 s) {
    float mean = s.x * (1.0f / 448), var = fmaxf(s.y * (1.0f / 448) - mean * mean, 0.0f);
    return f2{mean, __builtin_amdgcn_rsqf(var + 1e-5f)};
}
DI f2 ln_stat(const char* lds, int tok) { return ln_fin(*(const f2*)(lds + LDS_STAT + tok * 8)); }
template <int MODE>
DI void qkv_gemm(const u4 (&W)[28], u4 (&W2)[16], rsrc_t rs, int wo_off, char* lds, int v16, int m0, int t, int g, int xsrc, int T, int w) {
    constexpr int NM = MODE == 0 ? 4 : 3;
    f4 cs[2]; colsum<MODE == 2>(W, cs);
    BAR();
#pragma unroll
    for (int n = 0; n < 16; ++n) {
        int i = min(w + 8 * n, 125), s = i / 9, m = i - 9 * s, mt = max(8 * T - 1 + m, 0);
        dma(rs, lds, i * KB, v16, xsrc + (mt * 14 + s) * KB);
    }
    f4 acc[NM][2] = {}, sq[2] = {};
    const char* ap = lds + m0 * KB + v16;
    u4 b[2][NM];
#pragma unroll
    for (int s = 0; s < 14; ++s) {
        const bool first = s == 0 || s == 3 || s == 7 || s == 10, last = s == 2 || s == 6 || s == 9 || s == 13;
        if (s == 0) { WAITV(12); BAR(); } else if (s == 3) { WAITV(8); BAR(); } else if (s == 7) { WAITV(4); BAR(); } else if (s == 10) { WAITV(0); BAR(); }
#pragma unroll
        for (int mi = 0; mi < NM; ++mi) {
            if (first) b[s & 1][mi] = *(const u4*)(ap + (s * 9 + mi) * KB);
        }
#pragma unroll
        for (int mi = 0; mi < NM; ++mi) {
            if (!last) b[(s & 1) ^ 1][mi] = *(const u4*)(ap + (s * 9 + 9 + mi) * KB);
            u4 x = b[s & 1][mi];
            acc[mi][0] = MODE == 2 ? mfma(x, W[s], acc[mi][0]) : mfma(W[s], x, acc[mi][0]);
            acc[mi][1] = MODE == 2 ? mfma(x, W[14 + s], acc[mi][1]) : mfma(W[14 + s], x, acc[mi][1]);
            if (MODE == 1 && mi < 2) sq[mi] = mfma(x, x, sq[mi]);
            if (MODE == 2 && mi == 2) sq[0] = mfma(x, x, sq[0]);
        }
        if (s >= 10) {
#pragma unroll
            for (int i = 4 * (s - 10); i < 4 * (s - 9); ++i) W2[i] = __builtin_amdgcn_raw_buffer_load_b128(rs, v16 + (i & 3) * KB, wo_off + (i & ~3) * KB, 0);
        }
        __builtin_amdgcn_sched_barrier(0);
    }
    float* sp = (float*)(lds + LDS_STAT) + (m0 * 16 + t) * 2;
    if (MODE == 1) {
        if (g == 3) { sp[0] = acc[0][1][0]; sp[32] = acc[1][1][0]; sp[64] = acc[2][1][0]; }
        if (g == (t >> 2)) { sp[1] = diag(sq[0], t); sp[33] = diag(sq[1], t); }
    }
    if (MODE == 2) { if (g == (t >> 2)) sp[65] = diag(sq[0], t); }
    BAR();
#pragma unroll
    for (int mi = 0; mi < NM; ++mi) {
        f4 y0, y1;
        if (MODE < 2) {
            f2 st = ln_stat(lds, (m0 + mi) * 16 + t);
            y0 = (acc[mi][0] - cs[0] * st.x) * st.y; y1 = (acc[mi][1] - cs[1] * st.x) * st.y;
            if (g == 3) y1 = f4{0, 0, 0, 0};
        } else {
            const f4* p = (const f4*)(lds + LDS_STAT + ((m0 + mi) * 16 + 4 * g) * 8);
            f4 s01 = p[0], s23 = p[1];
            f2 st[4] = {ln_fin(f2{s01[0], s01[1]}), ln_fin(f2{s01[2], s01[3]}), ln_fin(f2{s23[0], s23[1]}), ln_fin(f2{s23[2], s23[3]})};
#pragma unroll
            for (int r = 0; r < 4; ++r) { y0[r] = (acc[mi][0][r] - cs[0][r] * st[r].x) * st[r].y; y1[r] = (acc[mi][1][r] - cs[1][r] * st[r].x) * st[r].y; }
        }
        *(u4*)(lds + (MODE * 9 + m0 + mi) * KB + v16) = u4{pk(y0[0], y0[1]), pk(y0[2], y0[3]), pk(y1[0], y1[1]), pk(y1[2], y1[3])};
    }
    BAR();
}
DI float max4g(float x) {
    u2 a = __builtin_amdgcn_permlane16_swap(fu(x), fu(x), false, false);
    x = fmaxf(uf(a.x), uf(a.y));
    u2 b = __builtin_amdgcn_permlane32_swap(fu(x), fu(x), false, false);
    return fmaxf(uf(b.x), uf(b.y));
}

__global__ __launch_bounds__(512, 2) void main_kernel(char* ws, char* out) {
    __shared__ __attribute__((aligned(1024))) char lds[LDS_BYTES];
    const rsrc_t rs = __builtin_amdgcn_make_buffer_rsrc(ws, 0, WS_BYTES, 0x00020000);
    gu32* cnt = (gu32*)(ws + O_CNT);
    const int w_ = __builtin_amdgcn_readfirstlane(threadIdx.x >> 6);
#define IDX() int tid = threadIdx.x, w = w_, bid = blockIdx.x; asm volatile("" : "+v"(tid)); asm volatile("" : "+s"(w), "+s"(bid)); \
    const int lane = tid & 63, t = lane & 15, g = lane >> 4, v16 = lane * 16, xc = bid & 7, sl = bid >> 3, w7 = w < 7 ? w : 6; \
    const int T = 4 * (xc >> 1) + (sl >> 3), j = 8 * (xc & 1) + (sl & 7);     \
    const int c2 = 2 * (xc & 1) + (sl & 1), sub2 = (sl >> 1) & 3;             \
    const int c4 = 4 * (xc & 1) + (sl & 3), half4 = (sl >> 2) & 1;            \
    const int p1 = w < 2 ? 0 : w < 5 ? 1 : 2, wq_off = O_WQKV + (j * 6 + 2 * p1) * 14 * KB; \
    (void)t; (void)g; (void)w7; (void)c2; (void)sub2; (void)c4; (void)half4; (void)wq_off; (void)T
    u4 W[28], W2[16];
    { IDX(); loadW<28>(W, rs, v16, wq_off); }
#pragma unroll 1
    for (int L = 0; L < 2; ++L) {
        const int xin = O_X + 2 * L * XSZ, xmid = xin + XSZ, xout = xmid + XSZ, ob = O_O + L * OSZ, hb = O_H + L * HSZ;
        gu32* cn = cnt + L * 4 * 32 * 64;
        {
            IDX();
            STAMP(0);
            const int m0 = w < 2 ? 1 + 4 * w : w < 5 ? 3 * (w - 2) : 3 * (w - 5);
            touchW<28>(W);
            if (L > 0 && w == 0) {
                int i = 2 * T - 1 + lane;
                if (lane < 3 && i >= 0) wait_cnt(cn - 32 * 64 + i * 64, 8);
            }
            STAMP(1);
            const int wo_off = O_WO + (7 * c2 + w7) * 16 * KB;
            undefW<16>(W2);
            if (opq(p1) == 0) { qkv_gemm<0>(W, W2, rs, wo_off, lds, v16, m0, t, g, xin, T, w); undefW<28>(W); }
            if (opq(p1) == 1) { qkv_gemm<1>(W, W2, rs, wo_off, lds, v16, m0, t, g, xin, T, w); undefW<28>(W); }
            if (opq(p1) == 2) qkv_gemm<2>(W, W2, rs, wo_off, lds, v16, m0, t, g, xin, T, w);
            STAMP(2);
            const char* qp = lds + (w + 1) * KB + v16;
            const u4 q = *(const u4*)qp, v0 = *(const u4*)(qp + 17 * KB), v1 = *(const u4*)(qp + 18 * KB);
            f4 lg[2] = {mfma(*(const u4*)(qp + 8 * KB), q, f4{0, 0, 0, 0}), mfma(*(const u4*)(qp + 9 * KB), q, f4{0, 0, 0, 0})};
            const int kmin = (T == 0 && w == 0) ? 16 : 0;
            float mx = -3.0e38f;
#pragma unroll
            for (int i = 0; i < 8; ++i) {
                int key = 16 * (i >> 2) + 4 * g + (i & 3), d = 16 + t - key;
                bool valid = d >= 0 && d <= 14 && !(d & 1) && key >= kmin;
                lg[i >> 2][i & 3] = valid ? lg[i >> 2][i & 3] * (0.18898223650461361f * 1.4426950408889634f) : -3.0e38f;
                mx = fmaxf(mx, lg[i >> 2][i & 3]);
            }
            mx = max4g(mx);
            float p[8], den = 0;
#pragma unroll
            for (int i = 0; i < 8; ++i) { p[i] = __builtin_amdgcn_exp2f(lg[i >> 2][i & 3] - mx); den += p[i]; }
            const float inv = __builtin_amdgcn_rcpf(sum4g(den));
            const u4 pb = {pk(p[0] * inv, p[1] * inv), pk(p[2] * inv, p[3] * inv), pk(p[4] * inv, p[5] * inv), pk(p[6] * inv, p[7] * inv)};
            const f4 o0 = mfma(u4{v0.x, v0.y, v1.x, v1.y}, pb, f4{0, 0, 0, 0}), o1 = mfma(u4{v0.z, v0.w, v1.z, v1.w}, pb, f4{0, 0, 0, 0});
            __builtin_amdgcn_raw_buffer_store_b128(u4{pk(o0[0], o0[1]), pk(o0[2], o0[3]), pk(o1[0], o1[1]), pk(o1[2], o1[3])}, rs, v16,
                                                   ob + ((8 * T + w) * 16 + j) * KB, SC1);
            STAMP(3);
            WAITV(0);
            touchW<16>(W2);
            BAR();
            if (tid == 448) signal(cn + T * 64);
        }
        {
            IDX();
            STAMP(4);
            const int mt0 = 8 * T + 2 * sub2;
            undefW<28>(W);
            if (opq(w) == 7) {
                if (lane == 0) wait_cnt(cn + T * 64, 16);
                asm volatile("" ::: "memory");
#pragma unroll
                for (int n = 0; n < 32; ++n) dma(rs, lds, n * KB, v16, ob + (mt0 * 16 + n) * KB);
                WAITV(0);
                BAR();
                BAR();
            }
            if (opq(w) != 7) {
                const int f = 112 * c2 + 16 * w + 8 * (g >> 1);
                const int xo = ((mt0 + (g & 1)) * 14 + (f >> 5)) * KB + (((f & 31) >> 3) * 16 + t) * 16;
                u4 r = __builtin_amdgcn_raw_buffer_load_b128(rs, xo, xin, SC1);
                loadW<28>(W, rs, v16, O_W13 + (7 * j + w) * 28 * KB);
                STAMP(5);
                BAR();
                STAMP(6);
                f4 a0 = {}, a1 = {};
#pragma unroll
                for (int s = 0; s < 16; ++s) {
                    a0 = mfma(W2[s], *(const u4*)(lds + s * KB + v16), a0);
                    a1 = mfma(W2[s], *(const u4*)(lds + (16 + s) * KB + v16), a1);
                }
                __builtin_amdgcn_raw_buffer_store_b128(resid_pack(a0, a1, r), rs, xo, xmid, SC1);
                STAMP(7);
                WAITV(0);
                BAR();
            }
            if (tid == 448) signal(cn + (32 + T) * 64);
        }
        {
            IDX();
            STAMP(8);
            f4 cs[2];
            colsum<false>(W, cs);
            if (tid == 448) wait_cnt(cn + (32 + T) * 64, 16);
            BAR();
            STAMP(9);
#pragma unroll
            for (int n = 0; n < 14; ++n) dma(rs, lds, (w + 8 * n) * KB, v16, xmid + (8 * T * 14 + w + 8 * n) * KB);
            const int f = 112 * j + 16 * w7 + 8 * (g >> 1);
            const int ho = ((8 * T + (g & 1)) * 56 + (f >> 5)) * KB + (((f & 31) >> 3) * 16 + t) * 16;
            const int w4 = O_W2 + (c4 * 56 + 7 * w) * 4 * KB;
#define LDW4(i) __builtin_amdgcn_raw_buffer_load_b128(rs, v16 + ((i) & 3) * KB, w4 + ((i) & ~3) * KB, 0)
#define P3WAIT(c) do { if (c == 0) WAITV(10); else if (c == 1) WAITV(7); else if (c == 2) WAITV(3); else if (c == 3) WAITV(8); BAR(); } while (0)
            undefW<8>(W2);
            if (opq(w) == 7) {
#pragma unroll
                for (int c = 0; c < 5; ++c) {
                    P3WAIT(c);
                    if (c < 4) {
                        f4 s1[2] = {}, s2[2] = {};
#pragma unroll
                        for (int s = 0; s < 14; ++s)
#pragma unroll
                            for (int mi = 0; mi < 2; ++mi) {
                                u4 b = *(const u4*)(lds + ((2 * c + mi) * 14 + s) * KB + v16);
                                s1[mi] = mfma(ONES, b, s1[mi]); s2[mi] = mfma(b, b, s2[mi]);
                            }
#pragma unroll
                        for (int mi = 0; mi < 2; ++mi)
                            if (g == (t >> 2)) *(f2*)(lds + LDS_STAT + ((2 * c + mi) * 16 + t) * 8) = f2{s1[mi][0], diag(s2[mi], t)};
                    }
                    if (c == 2) {
#pragma unroll
                        for (int i = 0; i < 8; ++i) W2[i] = LDW4(i);
                    }
                    if (c == 3) {
#pragma unroll
                        for (int i = 0; i < 20; ++i) W[i < 12 ? i : i + 2] = LDW4(8 + i);
                    }
                }
            }
            if (opq(w) != 7) {
                f4 a[2][2][2];
#pragma unroll
                for (int c = 0; c < 5; ++c) {
                    P3WAIT(c);
                    const char* ap = lds + 2 * c * 14 * KB + v16;
                    u4 b[2][2];
                    f2 st[2];
                    unsigned hp[2][2];
                    if (c < 4) { b[0][0] = *(const u4*)ap; b[0][1] = *(const u4*)(ap + 14 * KB); }
                    if (c > 0) { st[0] = ln_stat(lds, (2 * c - 2) * 16 + t); st[1] = ln_stat(lds, (2 * c - 1) * 16 + t); }
#pragma unroll
                    for (int s = 0; s < 14; ++s) {
                        if (c < 4) {
#pragma unroll
                            for (int mi = 0; mi < 2; ++mi) {
                                if (s < 13) b[(s & 1) ^ 1][mi] = *(const u4*)(ap + (mi * 14 + s + 1) * KB);
                                a[c & 1][mi][0] = mfma(W[s], b[s & 1][mi], s ? a[c & 1][mi][0] : f4{0, 0, 0, 0});
                                a[c & 1][mi][1] = mfma(W[14 + s], b[s & 1][mi], s ? a[c & 1][mi][1] : f4{0, 0, 0, 0});
                            }
                        }
                        if (c > 0 && s >= 1 && s <= 4) {
                            const int mi = (s - 1) >> 1, i0 = ((s - 1) & 1) * 2;
                            float h[2];
#pragma unroll
                            for (int i = 0; i < 2; ++i) {
                                float ga = (a[(c & 1) ^ 1][mi][0][i0 + i] - cs[0][i0 + i] * st[mi].x) * st[mi].y;
                                float up = (a[(c & 1) ^ 1][mi][1][i0 + i] - cs[1][i0 + i] * st[mi].x) * st[mi].y;
                                h[i] = ga * __builtin_amdgcn_rcpf(1.0f + __builtin_amdgcn_exp2f(ga * -1.4426950408889634f)) * up;
                            }
                            hp[mi][i0 >> 1] = pk(h[0], h[1]);
                            asm volatile("" : "+v"(hp[mi][i0 >> 1]));
                        }
                        if (c > 0 && s == 5) {
                            u2 s0 = __builtin_amdgcn_permlane16_swap(hp[0][0], hp[1][0], false, false);
                            u2 s1 = __builtin_amdgcn_permlane16_swap(hp[0][1], hp[1][1], false, false);
                            __builtin_amdgcn_raw_buffer_store_b128(u4{s0.x, s1.x, s0.y, s1.y}, rs, ho, hb + 2 * (c - 1) * 56 * KB, SC1);
                        }
                        if (c == 2 && s >= 6) W2[s - 6] = LDW4(s - 6);
                        if (c == 3 && s >= 1 && s <= 12) W[s - 1] = LDW4(7 + s);
                        if (c == 3 && s >= 6) W[s + 8] = LDW4(14 + s);
                        if (c < 4) __builtin_amdgcn_sched_barrier(0);
                    }
                    if (c == 3) STAMP(10);
                }
            }
            STAMP(11);
            WAITV(0);
            touchW<8>(W2); touchW<12>(W); touchW<22, 14>(W);
            BAR();
            if (tid == 0) signal(cn + (64 + T) * 64);
        }
        {
            IDX();
            STAMP(12);
            const int mt0 = 8 * T + 4 * half4;
            const int n4 = w & 3, mp = 2 * (w >> 2), f = 56 * c4 + 16 * n4 + 8 * (g >> 1), mt = mt0 + mp + (g & 1);
            const bool ok = n4 < 3 || g < 2;
            const int xo = ok ? (mt * 14 + (f >> 5)) * KB + (((f & 31) >> 3) * 16 + t) * 16 : 0;
            u4 r = __builtin_amdgcn_raw_buffer_load_b128(rs, xo, xmid, SC1);
            if (tid == 0) wait_cnt(cn + (64 + T) * 64, 16);
            BAR();
            STAMP(13);
            const int hsrc = hb + (mt0 * 56 + 7 * w) * KB;
            constexpr int NA = 18;
            u4 A[NA];
#define LDA(i) __builtin_amdgcn_raw_buffer_load_b128(rs, v16 + (((i) % 7) & 3) * KB, hsrc + (((i) / 7) * 56 + (((i) % 7) & 4)) * KB, SC1)
#pragma unroll
            for (int i = 0; i < NA; ++i) A[i] = LDA(i);
            f4 acc[2][4];
#pragma unroll
            for (int i = 0; i < 28; ++i) {
                const int m = i / 7, s = i % 7;
#pragma unroll
                for (int n = 0; n < 4; ++n) {
                    const int k = s * 4 + n;
                    acc[m & 1][n] = mfma(k < 8 ? W2[k] : W[k < 20 ? k - 8 : k - 6], A[i % NA], s ? acc[m & 1][n] : f4{0, 0, 0, 0});
                }
                if (i + NA < 28) A[i % NA] = LDA(i + NA);
                if (m > 0 && s >= 1 && s <= 4) *(f4*)(lds + (w * 16 + (m - 1) * 4 + s - 1) * KB + v16) = acc[(m & 1) ^ 1][s - 1];
                __builtin_amdgcn_sched_barrier(0);
            }
            STAMP(14);
#pragma unroll
            for (int n = 0; n < 4; ++n) *(f4*)(lds + (w * 16 + 12 + n) * KB + v16) = acc[1][n];
            BAR();
            f4 a0 = {}, a1 = {};
#pragma unroll
            for (int s = 0; s < 8; ++s) {
                a0 += *(const f4*)(lds + (s * 16 + mp * 4 + n4) * KB + v16);
                a1 += *(const f4*)(lds + (s * 16 + mp * 4 + 4 + n4) * KB + v16);
            }
            u4 y = resid_pack(a0, a1, r);
            STAMP(15);
            undefW<28>(W);
            if (opq(L) == 1) {
                const rsrc_t ro = __builtin_amdgcn_make_buffer_rsrc(out, 0, 2048 * 448 * 2, 0x00020000);
                if (ok) __builtin_amdgcn_raw_buffer_store_b128(y, ro, ((mt * 16 + t) * 448 + f) * 2, 0, 0);
            }
            if (opq(L) == 0) {
                if (ok) __builtin_amdgcn_raw_buffer_store_b128(y, rs, xo, xout, SC1);
                WAITV(0);
                BAR();
                if (tid == 0) signal(cn + (96 + 2 * T + half4) * 64);
                loadW<28>(W, rs, v16, wq_off);
            }
            STAMP(16);
        }
    }
}

extern "C" void kernel_launch(void* const* d_in, const int* in_sizes, int n_in, void* d_out, int out_size, void* d_ws, size_t ws_size,
                              hipStream_t stream) {
    typedef const unsigned short* cp;
    pack_kernel<<<532, 256, 0, stream>>>((cp)d_in[0], (cp)d_in[1], (cp)d_in[2], (cp)d_in[4], (cp)d_in[5], (cp)d_in[6], (char*)d_ws);
    main_kernel<<<256, 512, 0, stream>>>((char*)d_ws, (char*)d_out);
}
```

```cpp
#include <hip/hip_runtime.h>
#include <hip/hip_bf16.h>

typedef __attribute__((ext_vector_type(2))) __bf16 bf2;
typedef __attribute__((ext_vector_type(8))) __bf16 bf8;
typedef __attribute__((ext_vector_type(2))) float f2;
typedef __attribute__((ext_vector_type(4))) float f4;
typedef __attribute__((ext_vector_type(2))) unsigned u2;
typedef __attribute__((ext_vector_type(4))) unsigned u4;
typedef __attribute__((address_space(3))) void* ldsp;
typedef __attribute__((address_space(1))) unsigned gu32;
typedef __amdgpu_buffer_rsrc_t rsrc_t;
#define DI __device__ __forceinline__

constexpr int KB = 1024;
constexpr int O_WQKV = 0, O_WO = O_WQKV + 1344 * KB, O_W13 = O_WO + 448 * KB, O_W2 = O_W13 + 3136 * KB;
constexpr int O_X = O_W2 + 1792 * KB, XSZ = 1792 * KB;
constexpr int O_O = O_X + 4 * XSZ, OSZ = 2048 * KB;
constexpr int O_H = O_O + 2 * OSZ, HSZ = 7168 * KB;
constexpr int O_CNT = O_H + 2 * HSZ, CNT_WORDS = 8 * 32 * 64;
constexpr int O_DBG = O_CNT + CNT_WORDS * 4;
constexpr int WS_BYTES = O_DBG + 256 * 72 * 8;
constexpr int LDS_STAT = 128 * KB, LDS_CS = 130 * KB, LDS_BYTES = 132 * KB;
constexpr int SC1 = 16, NW1 = 16;

DI f4 mfma(u4 a, u4 b, f4 c) {
    return __builtin_amdgcn_mfma_f32_16x16x32_bf16(__builtin_bit_cast(bf8, a), __builtin_bit_cast(bf8, b), c, 0, 0, 0);
}
typedef __attribute__((ext_vector_type(16))) float f16v;
DI f16v mfma32(u4 a, u4 b, f16v c) {
    return __builtin_amdgcn_mfma_f32_32x32x16_bf16(__builtin_bit_cast(bf8, a), __builtin_bit_cast(bf8, b), c, 0, 0, 0);
}
DI unsigned pk(float a, float b) { f2 v = {a, b}; return __builtin_bit_cast(unsigned, __builtin_convertvector(v, bf2)); }
DI float lo(unsigned x) { return __builtin_bit_cast(float, x << 16); }
DI float hi(unsigned x) { return __builtin_bit_cast(float, x & 0xffff0000u); }
DI unsigned fu(float x) { return __builtin_bit_cast(unsigned, x); }
DI float uf(unsigned x) { return __builtin_bit_cast(float, x); }
DI float sum4g(float x) {
    u2 a = __builtin_amdgcn_permlane16_swap(fu(x), fu(x), false, false);
    x = uf(a.x) + uf(a.y);
    u2 b = __builtin_amdgcn_permlane32_swap(fu(x), fu(x), false, false);
    return uf(b.x) + uf(b.y);
}
#define WAITV(n) asm volatile("s_waitcnt vmcnt(" #n ")" ::: "memory")
#define BAR() do { asm volatile("s_waitcnt lgkmcnt(0)" ::: "memory"); __builtin_amdgcn_s_barrier(); asm volatile("" ::: "memory"); } while (0)
#define PSTAMP(i)
#define STAMP(i)
#define STAMPW(i, th)

__global__ __launch_bounds__(256) void pack_kernel(const unsigned short* x, const unsigned short* wqkv, const unsigned short* wo,
                                                   const unsigned short* w1, const unsigned short* w3, const unsigned short* w2, char* ws) {
    const int lane = threadIdx.x & 63, t = lane & 15, g = lane >> 4, q = t >> 2, c0 = 4 * (t & 3);
    int id = __builtin_amdgcn_readfirstlane(blockIdx.x * 4 + (threadIdx.x >> 6));
    if (blockIdx.x < CNT_WORDS / 256) ((unsigned*)(ws + O_CNT))[blockIdx.x * 256 + threadIdx.x] = 0;
    PSTAMP(0);
    if (id >= 1680) {
        id = (id - 1680) * 4;
        u4 v[4];
#pragma unroll
        for (int i = 0; i < 4; ++i) { int m = (id + i) / 14, s = (id + i) % 14; v[i] = *(const u4*)(x + (m * 16 + t) * 448 + s * 32 + g * 8); }
#pragma unroll
        for (int i = 0; i < 4; ++i) *(u4*)(ws + O_X + (id + i) * KB + lane * 16) = v[i];
        PSTAMP(1);
        return;
    }
    const unsigned short* src; int ld, col, row0, dst, rows = 448; bool ok = true, perm = false, perm2 = false, ones = false;
    if (id < 336) { int s = id / 24, nt = (id % 24) * 4 + q, p = nt >> 5, j = (nt & 31) >> 1, h = nt & 1;
        src = wqkv; ld = 1344; col = p * 448 + 28 * j + 16 * h + c0; ok = !(h && c0 == 12); ones = p == 1 && !ok; row0 = 32 * s;
        dst = O_WQKV + ((j * 6 + 2 * p + h) * 14 + s) * KB;
    } else if (id < 336 + 112) { id -= 336; int s = id / 7, nt = (id % 7) * 4 + q;
        src = wo; ld = 448; col = 16 * nt + c0; row0 = 28 * s; perm = true; dst = O_WO + (nt * 16 + s) * KB;
    } else if (id < 448 + 784) { id -= 448; int s = id / 56, r = id % 56, mat = r / 28, nt = (r % 28) * 4 + q;
        src = mat ? w3 : w1; ld = 1792; col = 16 * nt + c0; row0 = 32 * s; dst = O_W13 + ((nt * 2 + mat) * 14 + s) * KB;
    } else { id -= 448 + 784; int s = id >> 3, c = id & 7;
        src = w2; ld = 448; rows = 1792; perm2 = true; col = 56 * c + 16 * q + c0; ok = 16 * q + c0 < 56; row0 = 32 * s; dst = O_W2 + ((c * 56 + s) * 4 + q) * KB; }
    const rsrc_t r = __builtin_amdgcn_make_buffer_rsrc((void*)src, 0, rows * ld * 2, 0x00020000);
    u2 v[8];
#pragma unroll
    for (int j = 0; j < 8; ++j) {
        int k = perm ? (j < 4 ? 4 * g + j : 12 + 4 * g + j) : perm2 ? 16 * (g >> 1) + 4 * (g & 1) + (j < 4 ? j : j + 4) : 8 * g + j;
        v[j] = __builtin_amdgcn_raw_buffer_load_b64(r, ok && (!perm || k < 28) ? ((row0 + k) * ld + col) * 2 : 0x7ffffff0, 0, 0);
    }
#pragma unroll
    for (int c = 0; c < 4; ++c) {
        u4 o;
#pragma unroll
        for (int i = 0; i < 4; ++i) o[i] = __builtin_amdgcn_perm(v[2 * i + 1][c >> 1], v[2 * i][c >> 1], (c & 1) ? 0x07060302u : 0x05040100u);
        if (c == 0 && ones) o = u4{0x3f803f80u, 0x3f803f80u, 0x3f803f80u, 0x3f803f80u};
        *(u4*)(ws + dst + (g * 16 + c0 + c) * 16) = o;
    }
    PSTAMP(1);
}

DI void wait_cnt(gu32* p, unsigned want) {
#pragma unroll 1
    for (int i = 0; i < (1 << 21); ++i) {
        if (__hip_atomic_load(p, __ATOMIC_RELAXED, __HIP_MEMORY_SCOPE_AGENT) >= want) break;
        __builtin_amdgcn_s_sleep(1);
    }
}
DI void signal(gu32* p) { __hip_atomic_fetch_add(p, 1u, __ATOMIC_RELAXED, __HIP_MEMORY_SCOPE_AGENT); }
template <int N, int I0 = 0, int M> DI void loadW(u4 (&W)[M], rsrc_t rs, int v16, int base) {
#pragma unroll
    for (int i = I0; i < N; ++i) W[i] = __builtin_amdgcn_raw_buffer_load_b128(rs, v16 + (i & 3) * KB, base + (i & ~3) * KB, 0);
}
template <int N, int I0 = 0, int M> DI void touchW(u4 (&W)[M]) {
#pragma unroll
    for (int i = I0; i < N; ++i) asm volatile("" : "+v"(W[i]));
}
template <int N, int I0 = 0, int M> DI void undefW(u4 (&W)[M]) {
#pragma unroll
    for (int i = I0; i < N; ++i) asm volatile("" : "=v"(W[i]));
}
DI int opq(int x) { asm volatile("" : "+s"(x)); return x; }
DI void dma(rsrc_t rs, char* lds, int ldsoff, int v16, int goff) {
    __builtin_amdgcn_raw_ptr_buffer_load_lds(rs, (ldsp)(lds + ldsoff), 16, v16, goff, 0, SC1);
}
#define ONES u4{0x3f803f80u, 0x3f803f80u, 0x3f803f80u, 0x3f803f80u}
DI float diag(f4 d, int t) { float a = (t & 1) ? d[1] : d[0], b = (t & 1) ? d[3] : d[2]; return (t & 2) ? b : a; }
template <bool SWAP> DI void colsum(const u4 (&W)[28], f4 (&cs)[2]) {
    const u4 ones = {0x3f803f80u, 0x3f803f80u, 0x3f803f80u, 0x3f803f80u};
    cs[0] = f4{0, 0, 0, 0}; cs[1] = f4{0, 0, 0, 0};
#pragma unroll
    for (int s = 0; s < 14; ++s) {
        cs[0] = SWAP ? mfma(ones, W[s], cs[0]) : mfma(W[s], ones, cs[0]); cs[1] = SWAP ? mfma(ones, W[14 + s], cs[1]) : mfma(W[14 + s], ones, cs[1]);
    }
}
DI u4 resid_pack(f4 a0, f4 a1, u4 r) {
    float v[8];
#pragma unroll
    for (int i = 0; i < 4; ++i) { u2 s = __builtin_amdgcn_permlane16_swap(fu(a0[i]), fu(a1[i]), false, false); v[i] = uf(s.x); v[4 + i] = uf(s.y); }
    u4 o;
#pragma unroll
    for (int i = 0; i < 4; ++i) { unsigned y = pk(v[2 * i], v[2 * i + 1]); o[i] = pk(lo(r[i]) + lo(y), hi(r[i]) + hi(y)); }
    return o;
}
DI f2 ln_fin(f2 s) {
    float mean = s.x * (1.0f / 448), var = fmaxf(s.y * (1.0f / 448) - mean * mean, 0.0f);
    return f2{mean, __builtin_amdgcn_rsqf(var + 1e-5f)};
}
DI f2 ln_stat(const char* lds, int tok) { return ln_fin(*(const f2*)(lds + LDS_STAT + tok * 8)); }
template <int MODE>
DI void qkv_gemm(const u4 (&W)[28], u4 (&W2)[16], rsrc_t rs, int wo_off, char* lds, int v16, int m0, int t, int g, int xsrc, int T, int w) {
    constexpr int NM = MODE == 0 ? 4 : 3;
    f4 cs[2]; colsum<MODE == 2>(W, cs);
    BAR();
#pragma unroll
    for (int n = 0; n < 16; ++n) {
        int i = min(w + 8 * n, 125), s = i / 9, m = i - 9 * s, mt = max(8 * T - 1 + m, 0);
        dma(rs, lds, i * KB, v16, xsrc + (mt * 14 + s) * KB);
    }
    f4 acc[NM][2] = {}, sq[2] = {};
    const char* ap = lds + m0 * KB + v16;
    u4 b[2][NM];
#pragma unroll
    for (int s = 0; s < 14; ++s) {
        const bool first = s == 0 || s == 3 || s == 7 || s == 10, last = s == 2 || s == 6 || s == 9 || s == 13;
        if (s == 0) { WAITV(12); BAR(); } else if (s == 3) { WAITV(8); BAR(); } else if (s == 7) { WAITV(4); BAR(); } else if (s == 10) { WAITV(0); BAR(); }
#pragma unroll
        for (int mi = 0; mi < NM; ++mi) {
            if (first) b[s & 1][mi] = *(const u4*)(ap + (s * 9 + mi) * KB);
        }
#pragma unroll
        for (int mi = 0; mi < NM; ++mi) {
            if (!last) b[(s & 1) ^ 1][mi] = *(const u4*)(ap + (s * 9 + 9 + mi) * KB);
            u4 x = b[s & 1][mi];
            acc[mi][0] = MODE == 2 ? mfma(x, W[s], acc[mi][0]) : mfma(W[s], x, acc[mi][0]);
            acc[mi][1] = MODE == 2 ? mfma(x, W[14 + s], acc[mi][1]) : mfma(W[14 + s], x, acc[mi][1]);
            if (MODE == 1 && mi < 2) sq[mi] = mfma(x, x, sq[mi]);
            if (MODE == 2 && mi == 2) sq[0] = mfma(x, x, sq[0]);
        }
        if (s >= 10) {
#pragma unroll
            for (int i = 4 * (s - 10); i < 4 * (s - 9); ++i) W2[i] = __builtin_amdgcn_raw_buffer_load_b128(rs, v16 + (i & 3) * KB, wo_off + (i & ~3) * KB, 0);
        }
        __builtin_amdgcn_sched_barrier(0);
    }
    float* sp = (float*)(lds + LDS_STAT) + (m0 * 16 + t) * 2;
    if (MODE == 1) {
        if (g == 3) { sp[0] = acc[0][1][0]; sp[32] = acc[1][1][0]; sp[64] = acc[2][1][0]; }
        if (g == (t >> 2)) { sp[1] = diag(sq[0], t); sp[33] = diag(sq[1], t); }
    }
    if (MODE == 2) { if (g == (t >> 2)) sp[65] = diag(sq[0], t); }
    BAR();
#pragma unroll
    for (int mi = 0; mi < NM; ++mi) {
        f4 y0, y1;
        if (MODE < 2) {
            f2 st = ln_stat(lds, (m0 + mi) * 16 + t);
            y0 = (acc[mi][0] - cs[0] * st.x) * st.y; y1 = (acc[mi][1] - cs[1] * st.x) * st.y;
            if (g == 3) y1 = f4{0, 0, 0, 0};
        } else {
            const f4* p = (const f4*)(lds + LDS_STAT + ((m0 + mi) * 16 + 4 * g) * 8);
            f4 s01 = p[0], s23 = p[1];
            f2 st[4] = {ln_fin(f2{s01[0], s01[1]}), ln_fin(f2{s01[2], s01[3]}), ln_fin(f2{s23[0], s23[1]}), ln_fin(f2{s23[2], s23[3]})};
#pragma unroll
            for (int r = 0; r < 4; ++r) { y0[r] = (acc[mi][0][r] - cs[0][r] * st[r].x) * st[r].y; y1[r] = (acc[mi][1][r] - cs[1][r] * st[r].x) * st[r].y; }
        }
        *(u4*)(lds + (MODE * 9 + m0 + mi) * KB + v16) = u4{pk(y0[0], y0[1]), pk(y0[2], y0[3]), pk(y1[0], y1[1]), pk(y1[2], y1[3])};
    }
    BAR();
    touchW<16>(W2);
}
DI float max4g(float x) {
    u2 a = __builtin_amdgcn_permlane16_swap(fu(x), fu(x), false, false);
    x = fmaxf(uf(a.x), uf(a.y));
    u2 b = __builtin_amdgcn_permlane32_swap(fu(x), fu(x), false, false);
    return fmaxf(uf(b.x), uf(b.y));
}

__global__ __launch_bounds__(512, 2) void main_kernel(char* ws, char* out) {
    __shared__ __attribute__((aligned(1024))) char lds[LDS_BYTES];
    const rsrc_t rs = __builtin_amdgcn_make_buffer_rsrc(ws, 0, WS_BYTES, 0x00020000);
    gu32* cnt = (gu32*)(ws + O_CNT);
    const int w_ = __builtin_amdgcn_readfirstlane(threadIdx.x >> 6);
#define IDX() int tid = threadIdx.x, w = w_, bid = blockIdx.x; asm volatile("" : "+v"(tid)); asm volatile("" : "+s"(w), "+s"(bid)); \
    const int lane = tid & 63, t = lane & 15, g = lane >> 4, v16 = lane * 16, xc = bid & 7, sl = bid >> 3, w7 = w < 7 ? w : 6; \
    const int T = 4 * (xc >> 1) + (sl >> 3), j = 8 * (xc & 1) + (sl & 7);     \
    const int c2 = 2 * (xc & 1) + (sl & 1), sub2 = (sl >> 1) & 3;             \
    const int c4 = 4 * (xc & 1) + (sl & 3), half4 = (sl >> 2) & 1;            \
    const int p1 = w < 2 ? 0 : w < 5 ? 1 : 2, wq_off = O_WQKV + (j * 6 + 2 * p1) * 14 * KB; \
    (void)t; (void)g; (void)w7; (void)c2; (void)sub2; (void)c4; (void)half4; (void)wq_off; (void)T
    u4 W[28], W2[16];
    { IDX(); loadW<28>(W, rs, v16, wq_off); }
#pragma unroll 1
    for (int L = 0; L < 2; ++L) {
        const int xin = O_X + 2 * L * XSZ, xmid = xin + XSZ, xout = xmid + XSZ, ob = O_O + L * OSZ, hb = O_H + L * HSZ;
        gu32* cn = cnt + L * 4 * 32 * 64;
        {
            IDX();
            STAMP(0);
            const int m0 = w < 2 ? 1 + 4 * w : w < 5 ? 3 * (w - 2) : 3 * (w - 5);
            touchW<28>(W);
            if (L > 0 && w == 0) {
                int i = 2 * T - 1 + lane;
                if (lane < 3 && i >= 0) wait_cnt(cn - 32 * 64 + i * 64, 8);
            }
            STAMP(1);
            const int wo_off = O_WO + (7 * c2 + w7) * 16 * KB;
            undefW<16>(W2);
            if (opq(p1) == 0) { qkv_gemm<0>(W, W2, rs, wo_off, lds, v16, m0, t, g, xin, T, w); undefW<28>(W); }
            if (opq(p1) == 1) { qkv_gemm<1>(W, W2, rs, wo_off, lds, v16, m0, t, g, xin, T, w); undefW<28>(W); }
            if (opq(p1) == 2) qkv_gemm<2>(W, W2, rs, wo_off, lds, v16, m0, t, g, xin, T, w);
            STAMP(2);
            const char* qp = lds + (w + 1) * KB + v16;
            const u4 q = *(const u4*)qp, v0 = *(const u4*)(qp + 17 * KB), v1 = *(const u4*)(qp + 18 * KB);
            f4 lg[2] = {mfma(*(const u4*)(qp + 8 * KB), q, f4{0, 0, 0, 0}), mfma(*(const u4*)(qp + 9 * KB), q, f4{0, 0, 0, 0})};
            const int kmin = (T == 0 && w == 0) ? 16 : 0;
            float mx = -3.0e38f;
#pragma unroll
            for (int i = 0; i < 8; ++i) {
                int key = 16 * (i >> 2) + 4 * g + (i & 3), d = 16 + t - key;
                bool valid = d >= 0 && d <= 14 && !(d & 1) && key >= kmin;
                lg[i >> 2][i & 3] = valid ? lg[i >> 2][i & 3] * (0.18898223650461361f * 1.4426950408889634f) : -3.0e38f;
                mx = fmaxf(mx, lg[i >> 2][i & 3]);
            }
            mx = max4g(mx);
            float p[8], den = 0;
#pragma unroll
            for (int i = 0; i < 8; ++i) { p[i] = __builtin_amdgcn_exp2f(lg[i >> 2][i & 3] - mx); den += p[i]; }
            const float inv = __builtin_amdgcn_rcpf(sum4g(den));
            const u4 pb = {pk(p[0] * inv, p[1] * inv), pk(p[2] * inv, p[3] * inv), pk(p[4] * inv, p[5] * inv), pk(p[6] * inv, p[7] * inv)};
            const f4 o0 = mfma(u4{v0.x, v0.y, v1.x, v1.y}, pb, f4{0, 0, 0, 0}), o1 = mfma(u4{v0.z, v0.w, v1.z, v1.w}, pb, f4{0, 0, 0, 0});
            __builtin_amdgcn_raw_buffer_store_b128(u4{pk(o0[0], o0[1]), pk(o0[2], o0[3]), pk(o1[0], o1[1]), pk(o1[2], o1[3])}, rs, v16,
                                                   ob + ((8 * T + w) * 16 + j) * KB, SC1);
            STAMP(3);
            WAITV(0);
            BAR();
            if (tid == 448) signal(cn + T * 64);
        }
        {
            IDX();
            STAMP(4);
            const int mt0 = 8 * T + 2 * sub2;
            const int f = 112 * c2 + 16 * w7 + 8 * (g >> 1);
            const int xo = ((mt0 + (g & 1)) * 14 + (f >> 5)) * KB + (((f & 31) >> 3) * 16 + t) * 16;
            const int wv = (g & 1) * 14 * KB + (g >> 1) * 256 + t * 16, w13 = O_W13 + (7 * j + w7) * 28 * KB;
#define LDW13(i) __builtin_amdgcn_raw_buffer_load_b128(rs, wv + ((i) & 7) * 512, w13 + ((i) >> 3) * 4 * KB, 0)
            u4 r = {};
            f4 csum = {};
            const unsigned on = (t == (g & 1)) ? 0x3f803f80u : 0u;
            undefW<28>(W);
            if (opq(w) == 7) { if (lane == 0) wait_cnt(cn + T * 64, 16); }
            if (opq(w) != 7) {
                r = __builtin_amdgcn_raw_buffer_load_b128(rs, xo, xin, SC1);
#pragma unroll
                for (int i = 0; i < NW1; ++i) W[i] = LDW13(i);
            }
            STAMP(5);
            touchW<NW1>(W);
            asm volatile("" : "+v"(r));
            BAR();
#pragma unroll
            for (int n = 0; n < 4; ++n) dma(rs, lds, (w * 4 + n) * KB, v16, ob + (mt0 * 16 + w * 4 + n) * KB);
#pragma unroll
            for (int i = 0; i < NW1; ++i) csum = mfma(W[i], u4{on, on, on, on}, csum);
            WAITV(0);
            BAR();
            STAMP(6);
            if (opq(w) != 7) {
                f4 a0 = {}, a1 = {};
                u4 b[32];
#pragma unroll
                for (int s = 0; s < 32; ++s) b[s] = *(const u4*)(lds + ((s >> 1) + (s & 1) * 16) * KB + v16);
#pragma unroll
                for (int s = 0; s < 16; ++s) { a0 = mfma(W2[s], b[2 * s], a0); a1 = mfma(W2[s], b[2 * s + 1], a1); }
                __builtin_amdgcn_raw_buffer_store_b128(resid_pack(a0, a1, r), rs, xo, xmid, SC1);
            }
            STAMP(7);
            WAITV(0);
            BAR();
            if (tid == 448) signal(cn + (32 + T) * 64);
            STAMP(8);
            if (opq(w) == 7) { if (lane == 0) wait_cnt(cn + (32 + T) * 64, 16); }
            if (opq(w) != 7) {
#pragma unroll
                for (int i = NW1; i < 28; ++i) W[i] = LDW13(i);
            }
            touchW<28, NW1>(W);
            BAR();
            STAMP(9);
#pragma unroll
            for (int n = 0; n < 4; ++n) dma(rs, lds, (w + 8 * n) * KB, v16, xmid + (8 * T * 14 + w + 8 * n) * KB);
#pragma unroll
            for (int i = NW1; i < 28; ++i) csum = mfma(W[i], u4{on, on, on, on}, csum);
            if (t < 2) *(f4*)(lds + LDS_CS + w * 128 + t * 64 + g * 16) = csum;
        }
        {
            IDX();
            const int wv3 = (g & 1) * 14 * KB + (g >> 1) * 256 + t * 16;
            const int hk = lane >> 5, f = 112 * j + 16 * w7;
            const int ho = ((8 * T + (g & 1)) * 56 + (f >> 5)) * KB + ((((f & 31) >> 3) + hk) * 16 + t) * 16;
            const int w4 = O_W2 + (c4 * 56 + 7 * w) * 4 * KB, xs = xmid + (8 * T * 14 + w) * KB;
#define LDW4(i) __builtin_amdgcn_raw_buffer_load_b128(rs, v16 + ((i) & 3) * KB, w4 + ((i) & ~3) * KB, 0)
#define P3WAIT(c) do { if (c == 0) WAITV(0); else if (c == 1) WAITV(7); else if (c == 2) WAITV(3); else if (c == 3) WAITV(8); BAR(); } while (0)
            undefW<8>(W2);
            if (opq(w) == 7) {
#pragma unroll
                for (int c = 0; c < 5; ++c) {
                    P3WAIT(c);
                    if (c < 4) {
                        f4 s1[2] = {}, s2[2] = {};
                        u4 b[28];
#pragma unroll
                        for (int s = 0; s < 28; ++s) b[s] = *(const u4*)(lds + ((2 * c + (s & 1)) * 14 + (s >> 1)) * KB + v16);
#pragma unroll
                        for (int s = 0; s < 28; ++s) {
                            s1[s & 1] = mfma(ONES, b[s], s1[s & 1]); s2[s & 1] = mfma(b[s], b[s], s2[s & 1]);
                            if (c == 0 && s < 20 && !(s & 1)) dma(rs, lds, (w + 32 + 4 * s) * KB, v16, xs + (32 + 4 * s) * KB);
                        }
#pragma unroll
                        for (int mi = 0; mi < 2; ++mi)
                            if (g == (t >> 2)) *(f2*)(lds + LDS_STAT + ((2 * c + mi) * 16 + t) * 8) = ln_fin(f2{s1[mi][0], diag(s2[mi], t)});
                    }
                    STAMPW(27 + c, 448);
                    if (c == 2) {
#pragma unroll
                        for (int i = 0; i < 8; ++i) W2[i] = LDW4(i);
                    }
                    if (c == 3) {
#pragma unroll
                        for (int i = 0; i < 20; ++i) W[i] = LDW4(8 + i);
                    }
                }
                touchW<8>(W2); touchW<20>(W); undefW<28, 20>(W);
            }
            if (opq(w) != 7) {
                f16v a[2];
                f4 cs[4];
#pragma unroll
                for (int c = 0; c < 5; ++c) {
                    if (c) STAMPW(22 + c, 192);
                    P3WAIT(c);
                    STAMPW(17 + c, 192);
                    const char* ap = lds + 2 * c * 14 * KB + wv3;
                    u4 b[3];
                    f2 st;
                    float r1, r2;
                    unsigned hp[4];
                    if (c == 0) {
#pragma unroll
                        for (int i = 0; i < 4; ++i) cs[i] = *(const f4*)(lds + LDS_CS + w * 128 + i * 32 + hk * 16);
                    }
                    if (c < 4) { b[0] = *(const u4*)ap; b[1] = *(const u4*)(ap + 512); }
                    if (c > 0) { st = *(const f2*)(lds + LDS_STAT + ((2 * c - 2) * 16 + (lane & 31)) * 8); r1 = st.y * -1.4426950408889634f; r2 = st.y * st.y; }
                    float h0;
#pragma unroll
                    for (int s = 0; s < 28; ++s) {
                        if (c < 4) {
                            if (s < 26) b[(s + 2) % 3] = *(const u4*)(ap + (s + 2) * 512);
                            a[c & 1] = mfma32(W[s], b[s % 3], s ? a[c & 1] : f16v{});
                        }
                        if (c == 0 && s < 20 && !(s & 1)) dma(rs, lds, (w + 32 + 4 * s) * KB, v16, xs + (32 + 4 * s) * KB);
                        if (c > 0 && s >= 2 && s < 18 && !(s & 1)) {
                            const int i = (s - 2) >> 1;
                            float ga = __builtin_fmaf(-cs[i >> 2][i & 3], st.x, a[(c & 1) ^ 1][i]);
                            asm volatile("" : "+v"(ga));
                            float up = __builtin_fmaf(-cs[2 + (i >> 2)][i & 3], st.x, a[(c & 1) ^ 1][8 + i]);
                            float h = ga * up * (r2 * __builtin_amdgcn_rcpf(1.0f + __builtin_amdgcn_exp2f(ga * r1)));
                            if (i & 1) { hp[i >> 1] = pk(h0, h); asm volatile("" : "+v"(hp[i >> 1])); } else { h0 = h; asm volatile("" : "+v"(h0)); }
                        }
                        if (c > 0 && s == 18)
                            __builtin_amdgcn_raw_buffer_store_b128(u4{hp[0], hp[1], hp[2], hp[3]}, rs, ho, hb + 2 * (c - 1) * 56 * KB, SC1);
                        if (c == 2 && s >= 19 && s < 27) W2[s - 19] = LDW4(s - 19);
                        if (c == 3 && s >= 1 && s <= 20) W[s - 1] = LDW4(7 + s);
                        if (c < 4) __builtin_amdgcn_sched_barrier(0);
                    }
                    if (c == 3) STAMP(10);
                }
            }
            STAMP(11);
            WAITV(0);
            touchW<8>(W2); touchW<20>(W);
            BAR();
            if (tid == 0) signal(cn + (64 + T) * 64);
        }
        {
            IDX();
            STAMP(12);
            const int mt0 = 8 * T + 4 * half4;
            const int n4 = w & 3, mp = 2 * (w >> 2), f = 56 * c4 + 16 * n4 + 8 * (g >> 1), mt = mt0 + mp + (g & 1);
            const bool ok = n4 < 3 || g < 2;
            const int xo = ok ? (mt * 14 + (f >> 5)) * KB + (((f & 31) >> 3) * 16 + t) * 16 : 0;
            u4 r = __builtin_amdgcn_raw_buffer_load_b128(rs, xo, xmid, SC1);
            if (tid == 0) wait_cnt(cn + (64 + T) * 64, 16);
            BAR();
            STAMP(13);
            const int hsrc = hb + (mt0 * 56 + 7 * w) * KB;
            constexpr int NA = 18;
            u4 A[NA];
#define LDA(i) __builtin_amdgcn_raw_buffer_load_b128(rs, v16 + (((i) % 7) & 3) * KB, hsrc + (((i) / 7) * 56 + (((i) % 7) & 4)) * KB, SC1)
#pragma unroll
            for (int i = 0; i < NA; ++i) A[i] = LDA(i);
            f4 acc[2][4];
#pragma unroll
            for (int i = 0; i < 28; ++i) {
                const int m = i / 7, s = i % 7;
#pragma unroll
                for (int n = 0; n < 4; ++n) {
                    const int k = s * 4 + n;
                    acc[m & 1][n] = mfma(k < 8 ? W2[k] : W[k < 8 ? 0 : k - 8], A[i % NA], s ? acc[m & 1][n] : f4{0, 0, 0, 0});
                }
                if (i + NA < 28) A[i % NA] = LDA(i + NA);
                if (m > 0 && s >= 1 && s <= 4) *(f4*)(lds + (w * 16 + (m - 1) * 4 + s - 1) * KB + v16) = acc[(m & 1) ^ 1][s - 1];
                __builtin_amdgcn_sched_barrier(0);
            }
            STAMP(14);
#pragma unroll
            for (int n = 0; n < 4; ++n) *(f4*)(lds + (w * 16 + 12 + n) * KB + v16) = acc[1][n];
            BAR();
            f4 p[16];
#pragma unroll
            for (int s = 0; s < 16; ++s) p[s] = *(const f4*)(lds + ((s >> 1) * 16 + mp * 4 + (s & 1) * 4 + n4) * KB + v16);
            __builtin_amdgcn_sched_barrier(0);
            f4 a0 = p[0], a1 = p[1];
#pragma unroll
            for (int s = 1; s < 8; ++s) { a0 += p[2 * s]; a1 += p[2 * s + 1]; }
            u4 y = resid_pack(a0, a1, r);
            STAMP(15);
            undefW<28>(W);
            if (opq(L) == 1) {
                const rsrc_t ro = __builtin_amdgcn_make_buffer_rsrc(out, 0, 2048 * 448 * 2, 0x00020000);
                if (ok) __builtin_amdgcn_raw_buffer_store_b128(y, ro, ((mt * 16 + t) * 448 + f) * 2, 0, 0);
            }
            if (opq(L) == 0) {
                if (ok) __builtin_amdgcn_raw_buffer_store_b128(y, rs, xo, xout, SC1);
                WAITV(0);
                BAR();
                if (tid == 0) signal(cn + (96 + 2 * T + half4) * 64);
                loadW<28>(W, rs, v16, wq_off);
            }
            STAMP(16);
        }
    }
}

extern "C" void kernel_launch(void* const* d_in, const int* in_sizes, int n_in, void* d_out, int out_size, void* d_ws, size_t ws_size,
                              hipStream_t stream) {
    typedef const unsigned short* cp;
    pack_kernel<<<532, 256, 0, stream>>>((cp)d_in[0], (cp)d_in[1], (cp)d_in[2], (cp)d_in[4], (cp)d_in[5], (cp)d_in[6], (char*)d_ws);
    main_kernel<<<256, 512, 0, stream>>>((char*)d_ws, (char*)d_out);
}
```

```cpp
#include <hip/hip_runtime.h>
#include <hip/hip_bf16.h>

typedef __attribute__((ext_vector_type(2))) __bf16 bf2;
typedef __attribute__((ext_vector_type(8))) __bf16 bf8;
typedef __attribute__((ext_vector_type(2))) float f2;
typedef __attribute__((ext_vector_type(4))) float f4;
typedef __attribute__((ext_vector_type(2))) unsigned u2;
typedef __attribute__((ext_vector_type(4))) unsigned u4;
typedef __attribute__((address_space(3))) void* ldsp;
typedef __attribute__((address_space(1))) unsigned gu32;
typedef __amdgpu_buffer_rsrc_t rsrc_t;
#define DI __device__ __forceinline__

constexpr int KB = 1024;
constexpr int O_WQKV = 0, O_WO = O_WQKV + 1344 * KB, O_W13 = O_WO + 448 * KB, O_W2 = O_W13 + 3136 * KB;
constexpr int O_X = O_W2 + 1792 * KB, XSZ = 1792 * KB;
constexpr int O_O = O_X + 4 * XSZ, OSZ = 2048 * KB;
constexpr int O_H = O_O + 2 * OSZ, HSZ = 7168 * KB;
constexpr int O_CNT = O_H + 2 * HSZ, CNT_WORDS = 8 * 32 * 64;
constexpr int O_DBG = O_CNT + CNT_WORDS * 4;
constexpr int WS_BYTES = O_DBG + 256 * 72 * 8;
constexpr int LDS_STAT = 128 * KB, LDS_CS = 130 * KB, LDS_BYTES = 132 * KB;
constexpr int SC1 = 16, NW0 = 12, NW1 = 18, NW2 = 24;

DI f4 mfma(u4 a, u4 b, f4 c) {
    return __builtin_amdgcn_mfma_f32_16x16x32_bf16(__builtin_bit_cast(bf8, a), __builtin_bit_cast(bf8, b), c, 0, 0, 0);
}
typedef __attribute__((ext_vector_type(16))) float f16v;
DI f16v mfma32(u4 a, u4 b, f16v c) {
    return __builtin_amdgcn_mfma_f32_32x32x16_bf16(__builtin_bit_cast(bf8, a), __builtin_bit_cast(bf8, b), c, 0, 0, 0);
}
DI unsigned pk(float a, float b) { f2 v = {a, b}; return __builtin_bit_cast(unsigned, __builtin_convertvector(v, bf2)); }
DI float lo(unsigned x) { return __builtin_bit_cast(float, x << 16); }
DI float hi(unsigned x) { return __builtin_bit_cast(float, x & 0xffff0000u); }
DI unsigned fu(float x) { return __builtin_bit_cast(unsigned, x); }
DI float uf(unsigned x) { return __builtin_bit_cast(float, x); }
DI float sum4g(float x) {
    u2 a = __builtin_amdgcn_permlane16_swap(fu(x), fu(x), false, false);
    x = uf(a.x) + uf(a.y);
    u2 b = __builtin_amdgcn_permlane32_swap(fu(x), fu(x), false, false);
    return uf(b.x) + uf(b.y);
}
#define WAITV(n) asm volatile("s_waitcnt vmcnt(" #n ")" ::: "memory")
#define BAR() do { asm volatile("s_waitcnt lgkmcnt(0)" ::: "memory"); __builtin_amdgcn_s_barrier(); asm volatile("" ::: "memory"); } while (0)
#define PSTAMP(i)
#define STAMP(i)
#define STAMPW(i, th)

__global__ __launch_bounds__(256) void pack_kernel(const unsigned short* x, const unsigned short* wqkv, const unsigned short* wo,
                                                   const unsigned short* w1, const unsigned short* w3, const unsigned short* w2, char* ws) {
    const int lane = threadIdx.x & 63, t = lane & 15, g = lane >> 4, q = t >> 2, c0 = 4 * (t & 3);
    int id = __builtin_amdgcn_readfirstlane(blockIdx.x * 4 + (threadIdx.x >> 6));
    if (blockIdx.x < CNT_WORDS / 256) ((unsigned*)(ws + O_CNT))[blockIdx.x * 256 + threadIdx.x] = 0;
    PSTAMP(0);
    if (id >= 1680) {
        id = (id - 1680) * 4;
        u4 v[4];
#pragma unroll
        for (int i = 0; i < 4; ++i) { int m = (id + i) / 14, s = (id + i) % 14; v[i] = *(const u4*)(x + (m * 16 + t) * 448 + s * 32 + g * 8); }
#pragma unroll
        for (int i = 0; i < 4; ++i) *(u4*)(ws + O_X + (id + i) * KB + lane * 16) = v[i];
        PSTAMP(1);
        return;
    }
    const unsigned short* src; int ld, col, row0, dst, rows = 448; bool ok = true, perm = false, perm2 = false, ones = false;
    if (id < 336) { int s = id / 24, nt = (id % 24) * 4 + q, p = nt >> 5, j = (nt & 31) >> 1, h = nt & 1;
        src = wqkv; ld = 1344; col = p * 448 + 28 * j + 16 * h + c0; ok = !(h && c0 == 12); ones = p == 1 && !ok; row0 = 32 * s;
        dst = O_WQKV + ((j * 6 + 2 * p + h) * 14 + s) * KB;
    } else if (id < 336 + 112) { id -= 336; int s = id / 7, nt = (id % 7) * 4 + q;
        src = wo; ld = 448; col = 16 * nt + c0; row0 = 28 * s; perm = true; dst = O_WO + (nt * 16 + s) * KB;
    } else if (id < 448 + 784) { id -= 448; int s = id / 56, r = id % 56, mat = r / 28, nt = (r % 28) * 4 + q;
        src = mat ? w3 : w1; ld = 1792; col = 16 * nt + c0; row0 = 32 * s; dst = O_W13 + ((nt * 2 + mat) * 14 + s) * KB;
    } else { id -= 448 + 784; int s = id >> 3, c = id & 7;
        src = w2; ld = 448; rows = 1792; perm2 = true; col = 56 * c + 16 * q + c0; ok = 16 * q + c0 < 56; row0 = 32 * s; dst = O_W2 + ((c * 56 + s) * 4 + q) * KB; }
    const rsrc_t r = __builtin_amdgcn_make_buffer_rsrc((void*)src, 0, rows * ld * 2, 0x00020000);
    u2 v[8];
#pragma unroll
    for (int j = 0; j < 8; ++j) {
        int k = perm ? (j < 4 ? 4 * g + j : 12 + 4 * g + j) : perm2 ? 16 * (g >> 1) + 4 * (g & 1) + (j < 4 ? j : j + 4) : 8 * g + j;
        v[j] = __builtin_amdgcn_raw_buffer_load_b64(r, ok && (!perm || k < 28) ? ((row0 + k) * ld + col) * 2 : 0x7ffffff0, 0, 0);
    }
#pragma unroll
    for (int c = 0; c < 4; ++c) {
        u4 o;
#pragma unroll
        for (int i = 0; i < 4; ++i) o[i] = __builtin_amdgcn_perm(v[2 * i + 1][c >> 1], v[2 * i][c >> 1], (c & 1) ? 0x07060302u : 0x05040100u);
        if (c == 0 && ones) o = u4{0x3f803f80u, 0x3f803f80u, 0x3f803f80u, 0x3f803f80u};
        *(u4*)(ws + dst + (g * 16 + c0 + c) * 16) = o;
    }
    PSTAMP(1);
}

DI void wait_cnt(gu32* p, unsigned want) {
#pragma unroll 1
    for (int i = 0; i < (1 << 21); ++i) {
        if (__hip_atomic_load(p, __ATOMIC_RELAXED, __HIP_MEMORY_SCOPE_AGENT) >= want) break;
        __builtin_amdgcn_s_sleep(1);
    }
}
DI void signal(gu32* p) { __hip_atomic_fetch_add(p, 1u, __ATOMIC_RELAXED, __HIP_MEMORY_SCOPE_AGENT); }
template <int N, int I0 = 0, int M> DI void loadW(u4 (&W)[M], rsrc_t rs, int v16, int base) {
#pragma unroll
    for (int i = I0; i < N; ++i) W[i] = __builtin_amdgcn_raw_buffer_load_b128(rs, v16 + (i & 3) * KB, base + (i & ~3) * KB, 0);
}
template <int N, int I0 = 0, int M> DI void touchW(u4 (&W)[M]) {
#pragma unroll
    for (int i = I0; i < N; ++i) asm volatile("" : "+v"(W[i]));
}
template <int N, int I0 = 0, int M> DI void undefW(u4 (&W)[M]) {
#pragma unroll
    for (int i = I0; i < N; ++i) asm volatile("" : "=v"(W[i]));
}
DI int opq(int x) { asm volatile("" : "+s"(x)); return x; }
DI void dma(rsrc_t rs, char* lds, int ldsoff, int v16, int goff) {
    __builtin_amdgcn_raw_ptr_buffer_load_lds(rs, (ldsp)(lds + ldsoff), 16, v16, goff, 0, SC1);
}
#define ONES u4{0x3f803f80u, 0x3f803f80u, 0x3f803f80u, 0x3f803f80u}
DI float diag(f4 d, int t) { float a = (t & 1) ? d[1] : d[0], b = (t & 1) ? d[3] : d[2]; return (t & 2) ? b : a; }
template <bool SWAP> DI void colsum(const u4 (&W)[28], f4 (&cs)[2]) {
    const u4 ones = {0x3f803f80u, 0x3f803f80u, 0x3f803f80u, 0x3f803f80u};
    cs[0] = f4{0, 0, 0, 0}; cs[1] = f4{0, 0, 0, 0};
#pragma unroll
    for (int s = 0; s < 14; ++s) {
        cs[0] = SWAP ? mfma(ones, W[s], cs[0]) : mfma(W[s], ones, cs[0]); cs[1] = SWAP ? mfma(ones, W[14 + s], cs[1]) : mfma(W[14 + s], ones, cs[1]);
    }
}
DI u4 resid_pack(f4 a0, f4 a1, u4 r) {
    float v[8];
#pragma unroll
    for (int i = 0; i < 4; ++i) { u2 s = __builtin_amdgcn_permlane16_swap(fu(a0[i]), fu(a1[i]), false, false); v[i] = uf(s.x); v[4 + i] = uf(s.y); }
    u4 o;
#pragma unroll
    for (int i = 0; i < 4; ++i) { unsigned y = pk(v[2 * i], v[2 * i + 1]); o[i] = pk(lo(r[i]) + lo(y), hi(r[i]) + hi(y)); }
    return o;
}
DI f2 ln_fin(f2 s) {
    float mean = s.x * (1.0f / 448), var = fmaxf(s.y * (1.0f / 448) - mean * mean, 0.0f);
    return f2{mean, __builtin_amdgcn_rsqf(var + 1e-5f)};
}
DI f2 ln_stat(const char* lds, int tok) { return ln_fin(*(const f2*)(lds + LDS_STAT + tok * 8)); }
#define LDW13(i0, i1) do { const int wb = opq(w13); _Pragma("unroll") for (int i = i0; i < i1; ++i) \
                W[i] = __builtin_amdgcn_raw_buffer_load_b128(rs, wv + (i & 7) * 512, wb + (i >> 3) * 4 * KB, 0); } while (0)
template <int MODE>
DI void qkv_gemm(u4 (&W)[28], u4 (&W2)[16], rsrc_t rs, int wo_off, int w13, char* lds, int v16, int m0, int t, int g, int xsrc, int T, int w) {
    constexpr int NM = MODE == 0 ? 4 : 3;
#define XDMA(n) do { int i = min(w + 8 * (n), 125), s_ = i / 9, m = i - 9 * s_, mt = max(8 * T - 1 + m, 0); \
        dma(rs, lds, i * KB, v16, xsrc + (mt * 14 + s_) * KB); } while (0)
#pragma unroll
    for (int n = 0; n < 5; ++n) XDMA(n);
#pragma unroll
    for (int i = 0; i < 28; ++i) W[(i >> 1) + (i & 1) * 14] = *(const u4*)(lds + (44 + 28 * MODE + (i >> 1) + (i & 1) * 14) * KB + v16);
    __builtin_amdgcn_sched_barrier(0);
    f4 cs[2]; colsum<MODE == 2>(W, cs);
    f4 acc[NM][2] = {}, sq[2] = {};
    const char* ap = lds + m0 * KB + v16;
    u4 b[2][NM];
#pragma unroll
    for (int s = 0; s < 14; ++s) {
        const bool first = s == 0 || s == 3 || s == 7 || s == 10, last = s == 2 || s == 6 || s == 9 || s == 13;
        if (s == 0) { WAITV(1); BAR(); } else if (s == 3) { WAITV(6); BAR(); } else if (s == 7) { WAITV(10); BAR(); } else if (s == 10) { WAITV(12); BAR(); }
#pragma unroll
        for (int mi = 0; mi < NM; ++mi) {
            if (first) b[s & 1][mi] = *(const u4*)(ap + (s * 9 + mi) * KB);
        }
#pragma unroll
        for (int mi = 0; mi < NM; ++mi) {
            if (!last) b[(s & 1) ^ 1][mi] = *(const u4*)(ap + (s * 9 + 9 + mi) * KB);
            u4 x = b[s & 1][mi];
            acc[mi][0] = MODE == 2 ? mfma(x, W[s], acc[mi][0]) : mfma(W[s], x, acc[mi][0]);
            acc[mi][1] = MODE == 2 ? mfma(x, W[14 + s], acc[mi][1]) : mfma(W[14 + s], x, acc[mi][1]);
            if (MODE == 1 && mi < 2) sq[mi] = mfma(x, x, sq[mi]);
            if (MODE == 2 && mi == 2) sq[0] = mfma(x, x, sq[0]);
            if (mi < 3 && 5 + 3 * s + mi < 16) { XDMA(5 + 3 * s + mi); __builtin_amdgcn_sched_barrier(0); }
        }
        if (s >= 4 && s < 12) {
#pragma unroll
            for (int i = 2 * (s - 4); i < 2 * (s - 3); ++i) W2[i] = __builtin_amdgcn_raw_buffer_load_b128(rs, v16 + (i & 3) * KB, wo_off + (i & ~3) * KB, 0);
        }
        __builtin_amdgcn_sched_barrier(0);
    }
    float* sp = (float*)(lds + LDS_STAT) + (m0 * 16 + t) * 2;
    if (MODE == 1) {
        if (g == 3) { sp[0] = acc[0][1][0]; sp[32] = acc[1][1][0]; sp[64] = acc[2][1][0]; }
        if (g == (t >> 2)) { sp[1] = diag(sq[0], t); sp[33] = diag(sq[1], t); }
    }
    if (MODE == 2) { if (g == (t >> 2)) sp[65] = diag(sq[0], t); }
    BAR();
    const int wv = (g & 1) * 14 * KB + (g >> 1) * 256 + t * 16;
    LDW13(0, NW0);
#pragma unroll
    for (int mi = 0; mi < NM; ++mi) {
        f4 y0, y1;
        if (MODE < 2) {
            f2 st = ln_stat(lds, (m0 + mi) * 16 + t);
            y0 = (acc[mi][0] - cs[0] * st.x) * st.y; y1 = (acc[mi][1] - cs[1] * st.x) * st.y;
            if (g == 3) y1 = f4{0, 0, 0, 0};
        } else {
            const f4* p = (const f4*)(lds + LDS_STAT + ((m0 + mi) * 16 + 4 * g) * 8);
            f4 s01 = p[0], s23 = p[1];
            f2 st[4] = {ln_fin(f2{s01[0], s01[1]}), ln_fin(f2{s01[2], s01[3]}), ln_fin(f2{s23[0], s23[1]}), ln_fin(f2{s23[2], s23[3]})};
#pragma unroll
            for (int r = 0; r < 4; ++r) { y0[r] = (acc[mi][0][r] - cs[0][r] * st[r].x) * st[r].y; y1[r] = (acc[mi][1][r] - cs[1][r] * st[r].x) * st[r].y; }
        }
        *(u4*)(lds + (MODE * 9 + m0 + mi) * KB + v16) = u4{pk(y0[0], y0[1]), pk(y0[2], y0[3]), pk(y1[0], y1[1]), pk(y1[2], y1[3])};
    }
    BAR();
    touchW<16>(W2); touchW<NW0>(W);
}
DI float max4g(float x) {
    u2 a = __builtin_amdgcn_permlane16_swap(fu(x), fu(x), false, false);
    x = fmaxf(uf(a.x), uf(a.y));
    u2 b = __builtin_amdgcn_permlane32_swap(fu(x), fu(x), false, false);
    return fmaxf(uf(b.x), uf(b.y));
}

__global__ __launch_bounds__(512, 2) void main_kernel(char* ws, char* out) {
    __shared__ __attribute__((aligned(1024))) char lds[LDS_BYTES];
    const rsrc_t rs = __builtin_amdgcn_make_buffer_rsrc(ws, 0, WS_BYTES, 0x00020000);
    gu32* cnt = (gu32*)(ws + O_CNT);
    const int w_ = __builtin_amdgcn_readfirstlane(threadIdx.x >> 6);
#define IDX() int lane, w = w_, bid = blockIdx.x; asm volatile("v_mbcnt_lo_u32_b32 %0, -1, 0\n\tv_mbcnt_hi_u32_b32 %0, -1, %0" : "=v"(lane)); \
    asm volatile("" : "+s"(w), "+s"(bid)); \
    const int tid = w * 64 + lane, t = lane & 15, g = lane >> 4, v16 = lane * 16, xc = bid & 7, sl = bid >> 3, w7 = w < 7 ? w : 6; \
    const int T = 4 * (xc >> 1) + (sl >> 3), j = 8 * (xc & 1) + (sl & 7);     \
    const int c2 = 2 * (xc & 1) + (sl & 1), sub2 = (sl >> 1) & 3;             \
    const int c4 = 4 * (xc & 1) + (sl & 3), half4 = (sl >> 2) & 1;            \
    const int p1 = w < 2 ? 0 : w < 5 ? 1 : 2, wq_off = O_WQKV + (j * 6 + 2 * p1) * 14 * KB; \
    (void)t; (void)g; (void)w7; (void)c2; (void)sub2; (void)c4; (void)half4; (void)wq_off; (void)T
    u4 W[28], W2[16];
#pragma unroll 1
    for (int L = 0; L < 2; ++L) {
        const int xin = O_X + 2 * L * XSZ, xmid = xin + XSZ, xout = xmid + XSZ, ob = O_O + L * OSZ, hb = O_H + L * HSZ;
        gu32* cn = cnt + L * 4 * 32 * 64;
        {
            IDX();
            STAMP(0);
            const int m0 = w < 2 ? 1 + 4 * w : w < 5 ? 3 * (w - 2) : 3 * (w - 5);
#pragma unroll
            for (int n = 0; n < 11; ++n) { int i = min(w + 8 * n, 83); __builtin_amdgcn_raw_ptr_buffer_load_lds(rs, (ldsp)(lds + (44 + i) * KB), 16, v16, O_WQKV + (j * 84 + i) * KB, 0, 0); }
            if (L > 0 && w == 0) {
                int i = 2 * T - 1 + lane;
                if (lane < 3 && i >= 0) wait_cnt(cn - 32 * 64 + i * 64, 8);
            }
            WAITV(0);
            BAR();
            STAMP(1);
            const int wo_off = O_WO + (7 * c2 + w7) * 16 * KB, w13 = O_W13 + (7 * j + w7) * 28 * KB;
            undefW<16>(W2); undefW<NW0>(W);
            if (opq(p1) == 0) { qkv_gemm<0>(W, W2, rs, wo_off, w13, lds, v16, m0, t, g, xin, T, w); }
            if (opq(p1) == 1) { qkv_gemm<1>(W, W2, rs, wo_off, w13, lds, v16, m0, t, g, xin, T, w); }
            if (opq(p1) == 2) qkv_gemm<2>(W, W2, rs, wo_off, w13, lds, v16, m0, t, g, xin, T, w);
            STAMP(2);
            const char* qp = lds + (w + 1) * KB + v16;
            const u4 q = *(const u4*)qp, v0 = *(const u4*)(qp + 17 * KB), v1 = *(const u4*)(qp + 18 * KB);
            f4 lg[2] = {mfma(*(const u4*)(qp + 8 * KB), q, f4{0, 0, 0, 0}), mfma(*(const u4*)(qp + 9 * KB), q, f4{0, 0, 0, 0})};
            const int kmin = (T == 0 && w == 0) ? 16 : 0;
            float mx = -3.0e38f;
#pragma unroll
            for (int i = 0; i < 8; ++i) {
                int key = 16 * (i >> 2) + 4 * g + (i & 3), d = 16 + t - key;
                bool valid = d >= 0 && d <= 14 && !(d & 1) && key >= kmin;
                lg[i >> 2][i & 3] = valid ? lg[i >> 2][i & 3] * (0.18898223650461361f * 1.4426950408889634f) : -3.0e38f;
                mx = fmaxf(mx, lg[i >> 2][i & 3]);
            }
            mx = max4g(mx);
            float p[8], den = 0;
#pragma unroll
            for (int i = 0; i < 8; ++i) { p[i] = __builtin_amdgcn_exp2f(lg[i >> 2][i & 3] - mx); den += p[i]; }
            const float inv = __builtin_amdgcn_rcpf(sum4g(den));
            const u4 pb = {pk(p[0] * inv, p[1] * inv), pk(p[2] * inv, p[3] * inv), pk(p[4] * inv, p[5] * inv), pk(p[6] * inv, p[7] * inv)};
            const f4 o0 = mfma(u4{v0.x, v0.y, v1.x, v1.y}, pb, f4{0, 0, 0, 0}), o1 = mfma(u4{v0.z, v0.w, v1.z, v1.w}, pb, f4{0, 0, 0, 0});
            __builtin_amdgcn_raw_buffer_store_b128(u4{pk(o0[0], o0[1]), pk(o0[2], o0[3]), pk(o1[0], o1[1]), pk(o1[2], o1[3])}, rs, v16,
                                                   ob + ((8 * T + w) * 16 + j) * KB, SC1);
            STAMP(3);
            WAITV(0);
            BAR();
            if (tid == 448) signal(cn + T * 64);
        }
        {
            IDX();
            STAMP(4);
            const int mt0 = 8 * T + 2 * sub2;
            const int f = 112 * c2 + 16 * w7 + 8 * (g >> 1);
            const int xo = ((mt0 + (g & 1)) * 14 + (f >> 5)) * KB + (((f & 31) >> 3) * 16 + t) * 16;
            const int wv = (g & 1) * 14 * KB + (g >> 1) * 256 + t * 16, w13 = O_W13 + (7 * j + w7) * 28 * KB;
            u4 r = {};
            f4 csum = {};
            const unsigned on = (t == (g & 1)) ? 0x3f803f80u : 0u;
            undefW<28, NW0>(W);
            if (opq(w) == 7) { if (lane == 0) wait_cnt(cn + T * 64, 16); }
            if (opq(w) != 7) {
                r = __builtin_amdgcn_raw_buffer_load_b128(rs, xo, xin, SC1);
                LDW13(NW0, NW1);
            }
#pragma unroll
            for (int i = 0; i < NW0; ++i) csum = mfma(W[i], u4{on, on, on, on}, csum);
            STAMP(5);
            touchW<NW1, NW0>(W);
            asm volatile("" : "+v"(r));
            BAR();
#pragma unroll
            for (int n = 0; n < 4; ++n) dma(rs, lds, (w * 4 + n) * KB, v16, ob + (mt0 * 16 + w * 4 + n) * KB);
#pragma unroll
            for (int i = NW0; i < NW1; ++i) csum = mfma(W[i], u4{on, on, on, on}, csum);
            WAITV(0);
            BAR();
            STAMP(6);
            if (opq(w) != 7) {
                f4 a0 = {}, a1 = {};
                u4 b[12];
#pragma unroll
                for (int s = 0; s < 10; ++s) b[s] = *(const u4*)(lds + ((s >> 1) + (s & 1) * 16) * KB + v16);
#pragma unroll
                for (int s = 0; s < 16; ++s) {
                    if (s < 11) { b[(2 * s + 10) % 12] = *(const u4*)(lds + (s + 5) * KB + v16); b[(2 * s + 11) % 12] = *(const u4*)(lds + (s + 21) * KB + v16); }
                    a0 = mfma(W2[s], b[(2 * s) % 12], a0); a1 = mfma(W2[s], b[(2 * s + 1) % 12], a1);
                    if (NW1 + s < NW2) LDW13(NW1 + s, NW1 + s + 1);
                    __builtin_amdgcn_sched_barrier(0);
                }
                __builtin_amdgcn_raw_buffer_store_b128(resid_pack(a0, a1, r), rs, xo, xmid, SC1);
            }
            STAMP(7);
            WAITV(0);
            BAR();
            if (tid == 448) signal(cn + (32 + T) * 64);
            STAMP(8);
            if (opq(w) == 7) { if (lane == 0) wait_cnt(cn + (32 + T) * 64, 16); }
            if (opq(w) != 7) {
                LDW13(NW2, 28);
            }
#pragma unroll
            for (int i = NW1; i < NW2; ++i) csum = mfma(W[i], u4{on, on, on, on}, csum);
            touchW<28, NW2>(W);
            BAR();
            STAMP(9);
#pragma unroll
            for (int n = 0; n < 4; ++n) dma(rs, lds, (w + 8 * n) * KB, v16, xmid + (8 * T * 14 + w + 8 * n) * KB);
#pragma unroll
            for (int i = NW2; i < 28; ++i) csum = mfma(W[i], u4{on, on, on, on}, csum);
            if (t < 2) *(f4*)(lds + LDS_CS + w * 128 + t * 64 + g * 16) = csum;
        }
        {
            IDX();
            const int wv3 = (g & 1) * 14 * KB + (g >> 1) * 256 + t * 16;
            const int hk = lane >> 5, f = 112 * j + 16 * w7;
            const int ho = ((8 * T + (g & 1)) * 56 + (f >> 5)) * KB + ((((f & 31) >> 3) + hk) * 16 + t) * 16;
            const int w4 = O_W2 + (c4 * 56 + 7 * w) * 4 * KB, xs = xmid + (8 * T * 14 + w) * KB;
#define LDW4(i) __builtin_amdgcn_raw_buffer_load_b128(rs, v16 + ((i) & 3) * KB, w4 + ((i) & ~3) * KB, 0)
#define P3WAIT(c) do { if (c == 0) WAITV(0); else if (c == 1) WAITV(7); else if (c == 2) WAITV(11); else if (c == 3) WAITV(16); BAR(); } while (0)
            undefW<16>(W2);
            if (opq(w) == 7) {
#pragma unroll
                for (int c = 0; c < 5; ++c) {
                    P3WAIT(c);
                    if (c < 4) {
                        f4 s1[2] = {}, s2[2] = {};
                        u4 b[7];
#define HB(s) *(const u4*)(lds + ((2 * c + ((s) & 1)) * 14 + ((s) >> 1)) * KB + v16)
#pragma unroll
                        for (int s = 0; s < 6; ++s) b[s] = HB(s);
#pragma unroll
                        for (int s = 0; s < 28; ++s) {
                            if (s < 22) b[(s + 6) % 7] = HB(s + 6);
                            s1[s & 1] = mfma(ONES, b[s % 7], s1[s & 1]); s2[s & 1] = mfma(b[s % 7], b[s % 7], s2[s & 1]);
                            if (c == 0 && s < 20 && !(s & 1)) dma(rs, lds, (w + 32 + 4 * s) * KB, v16, xs + (32 + 4 * s) * KB);
                            if ((c == 1 || c == 2) && s % 3 == 1 && s < 24) W2[8 * (c - 1) + s / 3] = LDW4(8 * (c - 1) + s / 3);
                            if (c == 3 && s >= 2 && s <= 24 && !(s & 1)) W[s / 2 - 1] = LDW4(15 + s / 2);
                            __builtin_amdgcn_sched_barrier(0);
                        }
#pragma unroll
                        for (int mi = 0; mi < 2; ++mi)
                            if (g == (t >> 2)) *(f2*)(lds + LDS_STAT + ((2 * c + mi) * 16 + t) * 8) = ln_fin(f2{s1[mi][0], diag(s2[mi], t)});
                    }
                    STAMPW(27 + c, 448);
                }
                touchW<16>(W2); touchW<12>(W); undefW<28, 12>(W);
            }
            if (opq(w) != 7) {
                f16v a[2];
                f4 cs[4];
#pragma unroll
                for (int c = 0; c < 5; ++c) {
                    if (c) STAMPW(22 + c, 192);
                    P3WAIT(c);
                    STAMPW(17 + c, 192);
                    const char* ap = lds + 2 * c * 14 * KB + wv3;
                    u4 b[3];
                    f2 st;
                    float r1, r2;
                    unsigned hp[4];
                    if (c == 0) {
#pragma unroll
                        for (int i = 0; i < 4; ++i) cs[i] = *(const f4*)(lds + LDS_CS + w * 128 + i * 32 + hk * 16);
                    }
                    if (c < 4) { b[0] = *(const u4*)ap; b[1] = *(const u4*)(ap + 512); }
                    if (c > 0) { st = *(const f2*)(lds + LDS_STAT + ((2 * c - 2) * 16 + (lane & 31)) * 8); r1 = st.y * -1.4426950408889634f; r2 = st.y * st.y; }
                    float h0;
#pragma unroll
                    for (int s = 0; s < 28; ++s) {
                        if (c < 4) {
                            if (s < 26) b[(s + 2) % 3] = *(const u4*)(ap + (s + 2) * 512);
                            a[c & 1] = mfma32(W[s], b[s % 3], s ? a[c & 1] : f16v{});
                        }
                        if (c == 0 && s < 20 && !(s & 1)) dma(rs, lds, (w + 32 + 4 * s) * KB, v16, xs + (32 + 4 * s) * KB);
                        if (c > 0 && s >= 2 && s < 18 && !(s & 1)) {
                            const int i = (s - 2) >> 1;
                            float ga = __builtin_fmaf(-cs[i >> 2][i & 3], st.x, a[(c & 1) ^ 1][i]);
                            asm volatile("" : "+v"(ga));
                            float up = __builtin_fmaf(-cs[2 + (i >> 2)][i & 3], st.x, a[(c & 1) ^ 1][8 + i]);
                            float h = ga * up * (r2 * __builtin_amdgcn_rcpf(1.0f + __builtin_amdgcn_exp2f(ga * r1)));
                            if (i & 1) { hp[i >> 1] = pk(h0, h); asm volatile("" : "+v"(hp[i >> 1])); } else { h0 = h; asm volatile("" : "+v"(h0)); }
                        }
                        if (c > 0 && s == 18)
                            __builtin_amdgcn_raw_buffer_store_b128(u4{hp[0], hp[1], hp[2], hp[3]}, rs, ho, hb + 2 * (c - 1) * 56 * KB, SC1);
                        if ((c == 1 || c == 2) && s % 3 == 1 && s < 24) W2[8 * (c - 1) + s / 3] = LDW4(8 * (c - 1) + s / 3);
                        if (c == 3 && s >= 2 && s <= 24 && !(s & 1)) W[s / 2 - 1] = LDW4(15 + s / 2);
                        if (c < 4) __builtin_amdgcn_sched_barrier(0);
                    }
                    if (c == 3) STAMP(10);
                }
            }
            STAMP(11);
            WAITV(0);
            touchW<16>(W2); touchW<12>(W);
            BAR();
            if (tid == 0) signal(cn + (64 + T) * 64);
        }
        {
            IDX();
            STAMP(12);
            const int mt0 = 8 * T + 4 * half4;
            const int n4 = w & 3, mp = 2 * (w >> 2), f = 56 * c4 + 16 * n4 + 8 * (g >> 1), mt = mt0 + mp + (g & 1);
            const bool ok = n4 < 3 || g < 2;
            const int xo = ok ? (mt * 14 + (f >> 5)) * KB + (((f & 31) >> 3) * 16 + t) * 16 : 0;
            u4 r = __builtin_amdgcn_raw_buffer_load_b128(rs, xo, xmid, SC1);
            if (tid == 0) wait_cnt(cn + (64 + T) * 64, 16);
            BAR();
            STAMP(13);
            const int hsrc = hb + (mt0 * 56 + 7 * w) * KB;
            constexpr int NA = 18;
            u4 A[NA];
#define LDA(i) __builtin_amdgcn_raw_buffer_load_b128(rs, v16 + (((i) % 7) & 3) * KB, hsrc + (((i) / 7) * 56 + (((i) % 7) & 4)) * KB, SC1)
#pragma unroll
            for (int i = 0; i < NA; ++i) A[i] = LDA(i);
            f4 acc[2][4];
#pragma unroll
            for (int i = 0; i < 28; ++i) {
                const int m = i / 7, s = i % 7;
#pragma unroll
                for (int n = 0; n < 4; ++n) {
                    const int k = s * 4 + n;
                    acc[m & 1][n] = mfma(k < 16 ? W2[k & 15] : W[k < 16 ? 0 : k - 16], A[i % NA], s ? acc[m & 1][n] : f4{0, 0, 0, 0});
                }
                if (i + NA < 28) A[i % NA] = LDA(i + NA);
                if (m > 0 && s >= 1 && s <= 4) *(f4*)(lds + (w * 16 + (m - 1) * 4 + s - 1) * KB + v16) = acc[(m & 1) ^ 1][s - 1];
                __builtin_amdgcn_sched_barrier(0);
            }
            STAMP(14);
#pragma unroll
            for (int n = 0; n < 4; ++n) *(f4*)(lds + (w * 16 + 12 + n) * KB + v16) = acc[1][n];
            BAR();
            f4 p[16];
#pragma unroll
            for (int s = 0; s < 16; ++s) p[s] = *(const f4*)(lds + ((s >> 1) * 16 + mp * 4 + (s & 1) * 4 + n4) * KB + v16);
            __builtin_amdgcn_sched_barrier(0);
            f4 a0 = p[0], a1 = p[1];
#pragma unroll
            for (int s = 1; s < 8; ++s) { a0 += p[2 * s]; a1 += p[2 * s + 1]; }
            u4 y = resid_pack(a0, a1, r);
            STAMP(15);
            if (opq(L) == 1) {
                const rsrc_t ro = __builtin_amdgcn_make_buffer_rsrc(out, 0, 2048 * 448 * 2, 0x00020000);
                if (ok) __builtin_amdgcn_raw_buffer_store_b128(y, ro, ((mt * 16 + t) * 448 + f) * 2, 0, 0);
            }
            if (opq(L) == 0) {
                if (ok) __builtin_amdgcn_raw_buffer_store_b128(y, rs, xo, xout, SC1);
                WAITV(0);
                BAR();
                if (tid == 0) signal(cn + (96 + 2 * T + half4) * 64);
            }
            STAMP(16);
        }
    }
}

extern "C" void kernel_launch(void* const* d_in, const int* in_sizes, int n_in, void* d_out, int out_size, void* d_ws, size_t ws_size,
                              hipStream_t stream) {
    typedef const unsigned short* cp;
    pack_kernel<<<532, 256, 0, stream>>>((cp)d_in[0], (cp)d_in[1], (cp)d_in[2], (cp)d_in[4], (cp)d_in[5], (cp)d_in[6], (char*)d_ws);
    main_kernel<<<256, 512, 0, stream>>>((char*)d_ws, (char*)d_out);
}
```

```cpp
#include <hip/hip_runtime.h>
#include <hip/hip_bf16.h>

typedef __attribute__((ext_vector_type(2))) __bf16 bf2;
typedef __attribute__((ext_vector_type(8))) __bf16 bf8;
typedef __attribute__((ext_vector_type(2))) float f2;
typedef __attribute__((ext_vector_type(4))) float f4;
typedef __attribute__((ext_vector_type(2))) unsigned u2;
typedef __attribute__((ext_vector_type(4))) unsigned u4;
typedef __attribute__((address_space(3))) void* ldsp;
typedef __attribute__((address_space(1))) unsigned gu32;
typedef __amdgpu_buffer_rsrc_t rsrc_t;
#define DI __device__ __forceinline__

#define MAP 0
#define TRIM 0
constexpr int KB = 1024;
constexpr int O_WQKV = 0, O_WO = O_WQKV + 1344 * KB, O_W13 = O_WO + 448 * KB, O_W2 = O_W13 + 3136 * KB;
constexpr int O_X = O_W2 + 1792 * KB, XSZ = 1792 * KB;
constexpr int O_O = O_X + 4 * XSZ, OSZ = 2048 * KB;
constexpr int O_H = O_O + 2 * OSZ, HSZ = 7168 * KB;
constexpr int O_CNT = O_H + 2 * HSZ, CNT_WORDS = 8 * 32 * 64;
constexpr int O_DBG = O_CNT + CNT_WORDS * 4;
constexpr int WS_BYTES = O_DBG + 256 * 72 * 8;
constexpr int LDS_STAT = 128 * KB, LDS_CS = 130 * KB, LDS_BYTES = 132 * KB;
constexpr int SC1 = 16, NW0 = 12, NW1 = 18, NW2 = 24;

DI f4 mfma(u4 a, u4 b, f4 c) {
    return __builtin_amdgcn_mfma_f32_16x16x32_bf16(__builtin_bit_cast(bf8, a), __builtin_bit_cast(bf8, b), c, 0, 0, 0);
}
typedef __attribute__((ext_vector_type(16))) float f16v;
DI f16v mfma32(u4 a, u4 b, f16v c) {
    return __builtin_amdgcn_mfma_f32_32x32x16_bf16(__builtin_bit_cast(bf8, a), __builtin_bit_cast(bf8, b), c, 0, 0, 0);
}
DI unsigned pk(float a, float b) { f2 v = {a, b}; return __builtin_bit_cast(unsigned, __builtin_convertvector(v, bf2)); }
DI float lo(unsigned x) { return __builtin_bit_cast(float, x << 16); }
DI float hi(unsigned x) { return __builtin_bit_cast(float, x & 0xffff0000u); }
DI unsigned fu(float x) { return __builtin_bit_cast(unsigned, x); }
DI float uf(unsigned x) { return __builtin_bit_cast(float, x); }
DI float sum4g(float x) {
    u2 a = __builtin_amdgcn_permlane16_swap(fu(x), fu(x), false, false);
    x = uf(a.x) + uf(a.y);
    u2 b = __builtin_amdgcn_permlane32_swap(fu(x), fu(x), false, false);
    return uf(b.x) + uf(b.y);
}
#define WAITV(n) asm volatile("s_waitcnt vmcnt(" #n ")" ::: "memory")
#define BAR() do { asm volatile("s_waitcnt lgkmcnt(0)" ::: "memory"); __builtin_amdgcn_s_barrier(); asm volatile("" ::: "memory"); } while (0)
#define PSTAMP(i)
#define STAMP(i)
#define STAMPW(i, th)

#define PW 4
#define PAUX 0
__global__ __launch_bounds__(PW * 64) void pack_kernel(const unsigned short* x, const unsigned short* wqkv, const unsigned short* wo,
                                                   const unsigned short* w1, const unsigned short* w3, const unsigned short* w2, char* ws) {
    const int lane = threadIdx.x & 63, t = lane & 15, g = lane >> 4, q = t >> 2, c0 = 4 * (t & 3);
    int id = __builtin_amdgcn_readfirstlane(blockIdx.x * PW + (threadIdx.x >> 6));
    if (blockIdx.x < CNT_WORDS / (PW * 64)) ((unsigned*)(ws + O_CNT))[blockIdx.x * PW * 64 + threadIdx.x] = 0;
    const rsrc_t rw = __builtin_amdgcn_make_buffer_rsrc(ws, 0, WS_BYTES, 0x00020000);
    PSTAMP(0);
    if (id >= 1680) {
        id = (id - 1680) * 4;
        u4 v[4];
#pragma unroll
        for (int i = 0; i < 4; ++i) { int m = (id + i) / 14, s = (id + i) % 14; v[i] = *(const u4*)(x + (m * 16 + t) * 448 + s * 32 + g * 8); }
#pragma unroll
        for (int i = 0; i < 4; ++i) __builtin_amdgcn_raw_buffer_store_b128(v[i], rw, lane * 16, O_X + (id + i) * KB, PAUX);
        PSTAMP(1);
        return;
    }
    __shared__ __attribute__((aligned(16))) char tr[PW * 4 * KB];
    char* tp = tr + (threadIdx.x >> 6) * 4 * KB;
    const unsigned short* src; int ld, col, row0, dst, rows = 448; bool ok = true, perm = false, perm2 = false, ones = false;
    if (id < 336) { int s = id / 24, nt = (id % 24) * 4 + q, p = nt >> 5, j = (nt & 31) >> 1, h = nt & 1;
        src = wqkv; ld = 1344; col = p * 448 + 28 * j + 16 * h + c0; ok = !(h && c0 == 12); ones = p == 1 && !ok; row0 = 32 * s;
        dst = O_WQKV + ((j * 6 + 2 * p + h) * 14 + s) * KB;
    } else if (id < 336 + 112) { id -= 336; int s = id / 7, nt = (id % 7) * 4 + q;
        src = wo; ld = 448; col = 16 * nt + c0; row0 = 28 * s; perm = true; dst = O_WO + (nt * 16 + s) * KB;
    } else if (id < 448 + 784) { id -= 448; int s = id / 56, r = id % 56, mat = r / 28, nt = (r % 28) * 4 + q;
        src = mat ? w3 : w1; ld = 1792; col = 16 * nt + c0; row0 = 32 * s; dst = O_W13 + ((nt * 2 + mat) * 14 + s) * KB;
    } else { id -= 448 + 784; int s = id >> 3, c = id & 7;
        src = w2; ld = 448; rows = 1792; perm2 = true; col = 56 * c + 16 * q + c0; ok = 16 * q + c0 < 56; row0 = 32 * s; dst = O_W2 + ((c * 56 + s) * 4 + q) * KB; }
    const rsrc_t r = __builtin_amdgcn_make_buffer_rsrc((void*)src, 0, rows * ld * 2, 0x00020000);
    u2 v[8];
#pragma unroll
    for (int j = 0; j < 8; ++j) {
        int k = perm ? (j < 4 ? 4 * g + j : 12 + 4 * g + j) : perm2 ? 16 * (g >> 1) + 4 * (g & 1) + (j < 4 ? j : j + 4) : 8 * g + j;
        v[j] = __builtin_amdgcn_raw_buffer_load_b64(r, ok && (!perm || k < 28) ? ((row0 + k) * ld + col) * 2 : 0x7ffffff0, 0, 0);
    }
#pragma unroll
    for (int c = 0; c < 4; ++c) {
        u4 o;
#pragma unroll
        for (int i = 0; i < 4; ++i) o[i] = __builtin_amdgcn_perm(v[2 * i + 1][c >> 1], v[2 * i][c >> 1], (c & 1) ? 0x07060302u : 0x05040100u);
        if (c == 0 && ones) o = u4{0x3f803f80u, 0x3f803f80u, 0x3f803f80u, 0x3f803f80u};
        *(u4*)(tp + q * KB + (g * 16 + c0 + c) * 16) = o;
    }
#pragma unroll
    for (int c = 0; c < 4; ++c)
        __builtin_amdgcn_raw_buffer_store_b128(*(const u4*)(tp + c * KB + lane * 16), rw, lane * 16, __builtin_amdgcn_readlane(dst, 4 * c), PAUX);
    PSTAMP(1);
}

DI void wait_cnt(gu32* p, unsigned want) {
#pragma unroll 1
    for (int i = 0; i < (1 << 21); ++i) {
        if (__hip_atomic_load(p, __ATOMIC_RELAXED, __HIP_MEMORY_SCOPE_AGENT) >= want) break;
        __builtin_amdgcn_s_sleep(1);
    }
}
DI void signal(gu32* p) { __hip_atomic_fetch_add(p, 1u, __ATOMIC_RELAXED, __HIP_MEMORY_SCOPE_AGENT); }
template <int N, int I0 = 0, int M> DI void loadW(u4 (&W)[M], rsrc_t rs, int v16, int base) {
#pragma unroll
    for (int i = I0; i < N; ++i) W[i] = __builtin_amdgcn_raw_buffer_load_b128(rs, v16 + (i & 3) * KB, base + (i & ~3) * KB, 0);
}
template <int N, int I0 = 0, int M> DI void touchW(u4 (&W)[M]) {
#pragma unroll
    for (int i = I0; i < N; ++i) asm volatile("" : "+v"(W[i]));
}
template <int N, int I0 = 0, int M> DI void undefW(u4 (&W)[M]) {
#pragma unroll
    for (int i = I0; i < N; ++i) asm volatile("" : "=v"(W[i]));
}
DI int opq(int x) { asm volatile("" : "+s"(x)); return x; }
DI void dma(rsrc_t rs, char* lds, int ldsoff, int v16, int goff) {
    __builtin_amdgcn_raw_ptr_buffer_load_lds(rs, (ldsp)(lds + ldsoff), 16, v16, goff, 0, SC1);
}
#define ONES u4{0x3f803f80u, 0x3f803f80u, 0x3f803f80u, 0x3f803f80u}
DI float diag(f4 d, int t) { float a = (t & 1) ? d[1] : d[0], b = (t & 1) ? d[3] : d[2]; return (t & 2) ? b : a; }
template <bool SWAP> DI void colsum(const u4 (&W)[28], f4 (&cs)[2]) {
    const u4 ones = {0x3f803f80u, 0x3f803f80u, 0x3f803f80u, 0x3f803f80u};
    cs[0] = f4{0, 0, 0, 0}; cs[1] = f4{0, 0, 0, 0};
#pragma unroll
    for (int s = 0; s < 14; ++s) {
        cs[0] = SWAP ? mfma(ones, W[s], cs[0]) : mfma(W[s], ones, cs[0]); cs[1] = SWAP ? mfma(ones, W[14 + s], cs[1]) : mfma(W[14 + s], ones, cs[1]);
    }
}
DI u4 resid_pack(f4 a0, f4 a1, u4 r) {
    float v[8];
#pragma unroll
    for (int i = 0; i < 4; ++i) { u2 s = __builtin_amdgcn_permlane16_swap(fu(a0[i]), fu(a1[i]), false, false); v[i] = uf(s.x); v[4 + i] = uf(s.y); }
    u4 o;
#pragma unroll
    for (int i = 0; i < 4; ++i) { unsigned y = pk(v[2 * i], v[2 * i + 1]); o[i] = pk(lo(r[i]) + lo(y), hi(r[i]) + hi(y)); }
    return o;
}
DI f2 ln_fin(f2 s) {
    float mean = s.x * (1.0f / 448), var = fmaxf(s.y * (1.0f / 448) - mean * mean, 0.0f);
    return f2{mean, __builtin_amdgcn_rsqf(var + 1e-5f)};
}
DI f2 ln_stat(const char* lds, int tok) { return ln_fin(*(const f2*)(lds + LDS_STAT + tok * 8)); }
#define LDW13(i0, i1) do { const int wb = opq(w13); _Pragma("unroll") for (int i = i0; i < i1; ++i) \
                W[i] = __builtin_amdgcn_raw_buffer_load_b128(rs, wv + (i & 7) * 512, wb + (i >> 3) * 4 * KB, 0); } while (0)
template <int MODE>
DI void qkv_gemm(u4 (&W)[28], u4 (&W2)[16], rsrc_t rs, int wo_off, int w13, char* lds, int v16, int m0, int t, int g, int xsrc, int T, int w) {
    constexpr int NM = MODE == 0 ? 4 : 3;
#define XDMA(n) do { int i = min(w + 8 * (n), 125), s_ = i / 9, m = i - 9 * s_, mt = max(8 * T - 1 + m, 0); \
        dma(rs, lds, i * KB, v16, xsrc + (mt * 14 + s_) * KB); } while (0)
#pragma unroll
    for (int n = 0; n < 5; ++n) XDMA(n);
#pragma unroll
    for (int i = 0; i < 28; ++i) W[(i >> 1) + (i & 1) * 14] = *(const u4*)(lds + (44 + 28 * MODE + (i >> 1) + (i & 1) * 14) * KB + v16);
    __builtin_amdgcn_sched_barrier(0);
    f4 cs[2]; colsum<MODE == 2>(W, cs);
    const int vo = TRIM && MODE == 2 && w == 7 ? 0x7ff00000 : v16;
    f4 acc[NM][2] = {}, sq[2] = {};
    const char* ap = lds + m0 * KB + v16;
    u4 b[2][NM];
#pragma unroll
    for (int s = 0; s < 14; ++s) {
        const bool first = s == 0 || s == 3 || s == 7 || s == 10, last = s == 2 || s == 6 || s == 9 || s == 13;
        if (s == 0) { WAITV(1); BAR(); } else if (s == 3) { WAITV(6); BAR(); } else if (s == 7) { WAITV(10); BAR(); } else if (s == 10) { WAITV(12); BAR(); }
#pragma unroll
        for (int mi = 0; mi < NM; ++mi) {
            if (first) b[s & 1][mi] = *(const u4*)(ap + (s * 9 + mi) * KB);
        }
#pragma unroll
        for (int mi = 0; mi < NM; ++mi) {
            if (!last) b[(s & 1) ^ 1][mi] = *(const u4*)(ap + (s * 9 + 9 + mi) * KB);
            u4 x = b[s & 1][mi];
            acc[mi][0] = MODE == 2 ? mfma(x, W[s], acc[mi][0]) : mfma(W[s], x, acc[mi][0]);
            acc[mi][1] = MODE == 2 ? mfma(x, W[14 + s], acc[mi][1]) : mfma(W[14 + s], x, acc[mi][1]);
            if (MODE == 1 && mi < 2) sq[mi] = mfma(x, x, sq[mi]);
            if (MODE == 2 && mi == 2) sq[0] = mfma(x, x, sq[0]);
            if (mi < 3 && 5 + 3 * s + mi < 16) { XDMA(5 + 3 * s + mi); __builtin_amdgcn_sched_barrier(0); }
        }
        if (s >= 4 && s < 12) {
#pragma unroll
            for (int i = 2 * (s - 4); i < 2 * (s - 3); ++i) W2[i] = __builtin_amdgcn_raw_buffer_load_b128(rs, vo + (i & 3) * KB, wo_off + (i & ~3) * KB, 0);
        }
        __builtin_amdgcn_sched_barrier(0);
    }
    float* sp = (float*)(lds + LDS_STAT) + (m0 * 16 + t) * 2;
    if (MODE == 1) {
        if (g == 3) { sp[0] = acc[0][1][0]; sp[32] = acc[1][1][0]; sp[64] = acc[2][1][0]; }
        if (g == (t >> 2)) { sp[1] = diag(sq[0], t); sp[33] = diag(sq[1], t); }
    }
    if (MODE == 2) { if (g == (t >> 2)) sp[65] = diag(sq[0], t); }
    BAR();
    const int wv = TRIM && MODE == 2 && w == 7 ? 0x7ff00000 : (g & 1) * 14 * KB + (g >> 1) * 256 + t * 16;
    LDW13(0, NW0);
#pragma unroll
    for (int mi = 0; mi < NM; ++mi) {
        f4 y0, y1;
        if (MODE < 2) {
            f2 st = ln_stat(lds, (m0 + mi) * 16 + t);
            y0 = (acc[mi][0] - cs[0] * st.x) * st.y; y1 = (acc[mi][1] - cs[1] * st.x) * st.y;
            if (g == 3) y1 = f4{0, 0, 0, 0};
        } else {
            const f4* p = (const f4*)(lds + LDS_STAT + ((m0 + mi) * 16 + 4 * g) * 8);
            f4 s01 = p[0], s23 = p[1];
            f2 st[4] = {ln_fin(f2{s01[0], s01[1]}), ln_fin(f2{s01[2], s01[3]}), ln_fin(f2{s23[0], s23[1]}), ln_fin(f2{s23[2], s23[3]})};
#pragma unroll
            for (int r = 0; r < 4; ++r) { y0[r] = (acc[mi][0][r] - cs[0][r] * st[r].x) * st[r].y; y1[r] = (acc[mi][1][r] - cs[1][r] * st[r].x) * st[r].y; }
        }
        *(u4*)(lds + (MODE * 9 + m0 + mi) * KB + v16) = u4{pk(y0[0], y0[1]), pk(y0[2], y0[3]), pk(y1[0], y1[1]), pk(y1[2], y1[3])};
    }
    BAR();
    touchW<16>(W2); touchW<NW0>(W);
}
DI float max4g(float x) {
    u2 a = __builtin_amdgcn_permlane16_swap(fu(x), fu(x), false, false);
    x = fmaxf(uf(a.x), uf(a.y));
    u2 b = __builtin_amdgcn_permlane32_swap(fu(x), fu(x), false, false);
    return fmaxf(uf(b.x), uf(b.y));
}

__global__ __launch_bounds__(512, 2) void main_kernel(char* ws, char* out) {
    __shared__ __attribute__((aligned(1024))) char lds[LDS_BYTES];
    const rsrc_t rs = __builtin_amdgcn_make_buffer_rsrc(ws, 0, WS_BYTES, 0x00020000);
    gu32* cnt = (gu32*)(ws + O_CNT);
    const int w_ = __builtin_amdgcn_readfirstlane(threadIdx.x >> 6);
#define IDX() int lane, w = w_, bid = blockIdx.x; asm volatile("v_mbcnt_lo_u32_b32 %0, -1, 0\n\tv_mbcnt_hi_u32_b32 %0, -1, %0" : "=v"(lane)); \
    asm volatile("" : "+s"(w), "+s"(bid)); \
    const int tid = w * 64 + lane, t = lane & 15, g = lane >> 4, v16 = lane * 16, xc = bid & 7, sl = bid >> 3, w7 = w < 7 ? w : 6; \
    const int T = MAP ? 2 * xc + (sl >> 4) : 4 * (xc >> 1) + (sl >> 3), j = MAP ? sl & 15 : 8 * (xc & 1) + (sl & 7);     \
    const int c2 = MAP ? sl & 3 : 2 * (xc & 1) + (sl & 1), sub2 = MAP ? (sl >> 2) & 3 : (sl >> 1) & 3;             \
    const int c4 = MAP ? sl & 7 : 4 * (xc & 1) + (sl & 3), half4 = MAP ? (sl >> 3) & 1 : (sl >> 2) & 1;            \
    const int p1 = w < 2 ? 0 : w < 5 ? 1 : 2, wq_off = O_WQKV + (j * 6 + 2 * p1) * 14 * KB; \
    (void)t; (void)g; (void)w7; (void)c2; (void)sub2; (void)c4; (void)half4; (void)wq_off; (void)T
    u4 W[28], W2[16];
#pragma unroll 1
    for (int L = 0; L < 2; ++L) {
        const int xin = O_X + 2 * L * XSZ, xmid = xin + XSZ, xout = xmid + XSZ, ob = O_O + L * OSZ, hb = O_H + L * HSZ;
        gu32* cn = cnt + L * 4 * 32 * 64;
        {
            IDX();
            STAMP(0);
            const int m0 = w < 2 ? 1 + 4 * w : w < 5 ? 3 * (w - 2) : 3 * (w - 5);
#pragma unroll
            for (int n = 0; n < 11; ++n) { int i = min(w + 8 * n, 83); __builtin_amdgcn_raw_ptr_buffer_load_lds(rs, (ldsp)(lds + (44 + i) * KB), 16, v16, O_WQKV + (j * 84 + i) * KB, 0, 0); }
            if (L > 0 && w == 0) {
                int i = 2 * T - 1 + lane;
                if (lane < 3 && i >= 0) wait_cnt(cn - 32 * 64 + i * 64, 8);
            }
            WAITV(0);
            BAR();
            STAMP(1);
            const int wo_off = O_WO + (7 * c2 + w7) * 16 * KB, w13 = O_W13 + (7 * j + w7) * 28 * KB;
            undefW<16>(W2); undefW<NW0>(W);
            if (opq(p1) == 0) { qkv_gemm<0>(W, W2, rs, wo_off, w13, lds, v16, m0, t, g, xin, T, w); }
            if (opq(p1) == 1) { qkv_gemm<1>(W, W2, rs, wo_off, w13, lds, v16, m0, t, g, xin, T, w); }
            if (opq(p1) == 2) qkv_gemm<2>(W, W2, rs, wo_off, w13, lds, v16, m0, t, g, xin, T, w);
            STAMP(2);
            const char* qp = lds + (w + 1) * KB + v16;
            const u4 q = *(const u4*)qp, v0 = *(const u4*)(qp + 17 * KB), v1 = *(const u4*)(qp + 18 * KB);
            f4 lg[2] = {mfma(*(const u4*)(qp + 8 * KB), q, f4{0, 0, 0, 0}), mfma(*(const u4*)(qp + 9 * KB), q, f4{0, 0, 0, 0})};
            const int kmin = (T == 0 && w == 0) ? 16 : 0;
            float mx = -3.0e38f;
#pragma unroll
            for (int i = 0; i < 8; ++i) {
                int key = 16 * (i >> 2) + 4 * g + (i & 3), d = 16 + t - key;
                bool valid = d >= 0 && d <= 14 && !(d & 1) && key >= kmin;
                lg[i >> 2][i & 3] = valid ? lg[i >> 2][i & 3] * (0.18898223650461361f * 1.4426950408889634f) : -3.0e38f;
                mx = fmaxf(mx, lg[i >> 2][i & 3]);
            }
            mx = max4g(mx);
            float p[8], den = 0;
#pragma unroll
            for (int i = 0; i < 8; ++i) { p[i] = __builtin_amdgcn_exp2f(lg[i >> 2][i & 3] - mx); den += p[i]; }
            const float inv = __builtin_amdgcn_rcpf(sum4g(den));
            const u4 pb = {pk(p[0] * inv, p[1] * inv), pk(p[2] * inv, p[3] * inv), pk(p[4] * inv, p[5] * inv), pk(p[6] * inv, p[7] * inv)};
            const f4 o0 = mfma(u4{v0.x, v0.y, v1.x, v1.y}, pb, f4{0, 0, 0, 0}), o1 = mfma(u4{v0.z, v0.w, v1.z, v1.w}, pb, f4{0, 0, 0, 0});
            __builtin_amdgcn_raw_buffer_store_b128(u4{pk(o0[0], o0[1]), pk(o0[2], o0[3]), pk(o1[0], o1[1]), pk(o1[2], o1[3])}, rs, v16,
                                                   ob + ((8 * T + w) * 16 + j) * KB, SC1);
            STAMP(3);
            WAITV(0);
            BAR();
            if (tid == 448) signal(cn + T * 64);
        }
        {
            IDX();
            STAMP(4);
            const int mt0 = 8 * T + 2 * sub2;
            const int f = 112 * c2 + 16 * w7 + 8 * (g >> 1);
            const int xo = ((mt0 + (g & 1)) * 14 + (f >> 5)) * KB + (((f & 31) >> 3) * 16 + t) * 16;
            const int wv = (g & 1) * 14 * KB + (g >> 1) * 256 + t * 16, w13 = O_W13 + (7 * j + w7) * 28 * KB;
            u4 r = {};
            f4 csum = {};
            const unsigned on = (t == (g & 1)) ? 0x3f803f80u : 0u;
            undefW<28, NW0>(W);
            if (opq(w) == 7) { if (lane == 0) wait_cnt(cn + T * 64, 16); }
            if (opq(w) != 7) {
                r = __builtin_amdgcn_raw_buffer_load_b128(rs, xo, xin, SC1);
                LDW13(NW0, NW1);
            }
#pragma unroll
            for (int i = 0; i < NW0; ++i) csum = mfma(W[i], u4{on, on, on, on}, csum);
            STAMP(5);
            touchW<NW1, NW0>(W);
            asm volatile("" : "+v"(r));
            BAR();
#pragma unroll
            for (int n = 0; n < 4; ++n) dma(rs, lds, (w * 4 + n) * KB, v16, ob + (mt0 * 16 + w * 4 + n) * KB);
#pragma unroll
            for (int i = NW0; i < NW1; ++i) csum = mfma(W[i], u4{on, on, on, on}, csum);
            WAITV(0);
            BAR();
            STAMP(6);
            if (opq(w) != 7) {
                f4 a0 = {}, a1 = {};
                u4 b[12];
#pragma unroll
                for (int s = 0; s < 10; ++s) b[s] = *(const u4*)(lds + ((s >> 1) + (s & 1) * 16) * KB + v16);
#pragma unroll
                for (int s = 0; s < 16; ++s) {
                    if (s < 11) { b[(2 * s + 10) % 12] = *(const u4*)(lds + (s + 5) * KB + v16); b[(2 * s + 11) % 12] = *(const u4*)(lds + (s + 21) * KB + v16); }
                    a0 = mfma(W2[s], b[(2 * s) % 12], a0); a1 = mfma(W2[s], b[(2 * s + 1) % 12], a1);
                    if (NW1 + s < NW2) LDW13(NW1 + s, NW1 + s + 1);
                    __builtin_amdgcn_sched_barrier(0);
                }
                __builtin_amdgcn_raw_buffer_store_b128(resid_pack(a0, a1, r), rs, xo, xmid, SC1);
            }
            STAMP(7);
            WAITV(0);
            BAR();
            if (tid == 448) signal(cn + (32 + T) * 64);
            STAMP(8);
            if (opq(w) == 7) { if (lane == 0) wait_cnt(cn + (32 + T) * 64, 16); }
            if (opq(w) != 7) {
                LDW13(NW2, 28);
            }
#pragma unroll
            for (int i = NW1; i < NW2; ++i) csum = mfma(W[i], u4{on, on, on, on}, csum);
            touchW<28, NW2>(W);
            BAR();
            STAMP(9);
#pragma unroll
            for (int n = 0; n < 4; ++n) dma(rs, lds, (w + 8 * n) * KB, v16, xmid + (8 * T * 14 + w + 8 * n) * KB);
#pragma unroll
            for (int i = NW2; i < 28; ++i) csum = mfma(W[i], u4{on, on, on, on}, csum);
            if (t < 2) *(f4*)(lds + LDS_CS + w * 128 + t * 64 + g * 16) = csum;
        }
        {
            IDX();
            const int wv3 = (g & 1) * 14 * KB + (g >> 1) * 256 + t * 16;
            const int hk = lane >> 5, f = 112 * j + 16 * w7;
            const int ho = ((8 * T + (g & 1)) * 56 + (f >> 5)) * KB + ((((f & 31) >> 3) + hk) * 16 + t) * 16;
            const int w4 = O_W2 + (c4 * 56 + 7 * w) * 4 * KB, xs = xmid + (8 * T * 14 + w) * KB;
            const int v16p = TRIM && t >= 8 ? 0x7ff00000 : v16;
#define LDW4(i) __builtin_amdgcn_raw_buffer_load_b128(rs, (((i) & 3) == 3 ? v16p : v16) + ((i) & 3) * KB, w4 + ((i) & ~3) * KB, 0)
#define P3WAIT(c) do { if (c == 0) WAITV(0); else if (c == 1) WAITV(7); else if (c == 2) WAITV(11); else if (c == 3) WAITV(16); BAR(); } while (0)
            undefW<16>(W2);
            if (opq(w) == 7) {
#pragma unroll
                for (int c = 0; c < 5; ++c) {
                    P3WAIT(c);
                    if (c < 4) {
                        f4 s1[2] = {}, s2[2] = {};
                        u4 b[7];
#define HB(s) *(const u4*)(lds + ((2 * c + ((s) & 1)) * 14 + ((s) >> 1)) * KB + v16)
#pragma unroll
                        for (int s = 0; s < 6; ++s) b[s] = HB(s);
#pragma unroll
                        for (int s = 0; s < 28; ++s) {
                            if (s < 22) b[(s + 6) % 7] = HB(s + 6);
                            s1[s & 1] = mfma(ONES, b[s % 7], s1[s & 1]); s2[s & 1] = mfma(b[s % 7], b[s % 7], s2[s & 1]);
                            if (c == 0 && s < 20 && !(s & 1)) dma(rs, lds, (w + 32 + 4 * s) * KB, v16, xs + (32 + 4 * s) * KB);
                            if ((c == 1 || c == 2) && s % 3 == 1 && s < 24) W2[8 * (c - 1) + s / 3] = LDW4(8 * (c - 1) + s / 3);
                            if (c == 3 && s >= 2 && s <= 24 && !(s & 1)) W[s / 2 - 1] = LDW4(15 + s / 2);
                            __builtin_amdgcn_sched_barrier(0);
                        }
#pragma unroll
                        for (int mi = 0; mi < 2; ++mi)
                            if (g == (t >> 2)) *(f2*)(lds + LDS_STAT + ((2 * c + mi) * 16 + t) * 8) = ln_fin(f2{s1[mi][0], diag(s2[mi], t)});
                    }
                }
                touchW<16>(W2); touchW<12>(W); undefW<28, 12>(W);
            }
            if (opq(w) != 7) {
                f16v a[2];
                f4 cs[4];
#pragma unroll
                for (int c = 0; c < 5; ++c) {
                    P3WAIT(c);
                    const char* ap = lds + 2 * c * 14 * KB + wv3;
                    u4 b[3];
                    f2 st;
                    float r1, r2;
                    unsigned hp[4];
                    if (c == 0) {
#pragma unroll
                        for (int i = 0; i < 4; ++i) cs[i] = *(const f4*)(lds + LDS_CS + w * 128 + i * 32 + hk * 16);
                    }
                    if (c < 4) { b[0] = *(const u4*)ap; b[1] = *(const u4*)(ap + 512); }
                    if (c > 0) { st = *(const f2*)(lds + LDS_STAT + ((2 * c - 2) * 16 + (lane & 31)) * 8); r1 = st.y * -1.4426950408889634f; r2 = st.y * st.y; }
                    float h0;
#pragma unroll
                    for (int s = 0; s < 28; ++s) {
                        if (c < 4) {
                            if (s < 26) b[(s + 2) % 3] = *(const u4*)(ap + (s + 2) * 512);
                            a[c & 1] = mfma32(W[s], b[s % 3], s ? a[c & 1] : f16v{});
                        }
                        if (c == 0 && s < 20 && !(s & 1)) dma(rs, lds, (w + 32 + 4 * s) * KB, v16, xs + (32 + 4 * s) * KB);
                        if (c > 0 && s >= 2 && s < 18 && !(s & 1)) {
                            const int i = (s - 2) >> 1;
                            float ga = __builtin_fmaf(-cs[i >> 2][i & 3], st.x, a[(c & 1) ^ 1][i]);
                            asm volatile("" : "+v"(ga));
                            float up = __builtin_fmaf(-cs[2 + (i >> 2)][i & 3], st.x, a[(c & 1) ^ 1][8 + i]);
                            float gu = ga * up;
                            asm volatile("" : "+v"(gu));
                            float h = gu * (r2 * __builtin_amdgcn_rcpf(1.0f + __builtin_amdgcn_exp2f(ga * r1)));
                            if (i & 1) { hp[i >> 1] = pk(h0, h); asm volatile("" : "+v"(hp[i >> 1])); } else { h0 = h; asm volatile("" : "+v"(h0)); }
                        }
                        if (c > 0 && s == 18)
                            __builtin_amdgcn_raw_buffer_store_b128(u4{hp[0], hp[1], hp[2], hp[3]}, rs, ho, hb + 2 * (c - 1) * 56 * KB, SC1);
                        if ((c == 1 || c == 2) && s % 3 == 1 && s < 24) W2[8 * (c - 1) + s / 3] = LDW4(8 * (c - 1) + s / 3);
                        if (c == 3 && s >= 2 && s <= 24 && !(s & 1)) W[s / 2 - 1] = LDW4(15 + s / 2);
                        if (c < 4) __builtin_amdgcn_sched_barrier(0);
                    }
                    if (c == 3) STAMP(10);
                }
            }
            STAMP(11);
            WAITV(0);
            touchW<16>(W2); touchW<12>(W);
            BAR();
            if (tid == 0) signal(cn + (64 + T) * 64);
        }
        {
            IDX();
            STAMP(12);
            const int mt0 = 8 * T + 4 * half4;
            const int n4 = w & 3, mp = 2 * (w >> 2), f = 56 * c4 + 16 * n4 + 8 * (g >> 1), mt = mt0 + mp + (g & 1);
            const bool ok = n4 < 3 || g < 2;
            const int xo = ok ? (mt * 14 + (f >> 5)) * KB + (((f & 31) >> 3) * 16 + t) * 16 : 0;
            u4 r = __builtin_amdgcn_raw_buffer_load_b128(rs, xo, xmid, SC1);
            if (tid == 0) wait_cnt(cn + (64 + T) * 64, 16);
            BAR();
            STAMP(13);
            const int hsrc = hb + (mt0 * 56 + 7 * w) * KB;
            constexpr int NA = 18;
            u4 A[NA];
#define LDA(i) __builtin_amdgcn_raw_buffer_load_b128(rs, v16 + (((i) % 7) & 3) * KB, hsrc + (((i) / 7) * 56 + (((i) % 7) & 4)) * KB, SC1)
#pragma unroll
            for (int i = 0; i < NA; ++i) A[i] = LDA(i);
            f4 acc[2][4];
#pragma unroll
            for (int i = 0; i < 28; ++i) {
                const int m = i / 7, s = i % 7;
#pragma unroll
                for (int n = 0; n < 4; ++n) {
                    const int k = s * 4 + n;
                    acc[m & 1][n] = mfma(k < 16 ? W2[k & 15] : W[k < 16 ? 0 : k - 16], A[i % NA], s ? acc[m & 1][n] : f4{0, 0, 0, 0});
                }
                if (i + NA < 28) A[i % NA] = LDA(i + NA);
                if (m > 0 && s >= 1 && s <= 4) *(f4*)(lds + (w * 16 + (m - 1) * 4 + s - 1) * KB + v16) = acc[(m & 1) ^ 1][s - 1];
                __builtin_amdgcn_sched_barrier(0);
            }
            STAMP(14);
#pragma unroll
            for (int n = 0; n < 4; ++n) *(f4*)(lds + (w * 16 + 12 + n) * KB + v16) = acc[1][n];
            BAR();
            f4 p[16];
#pragma unroll
            for (int s = 0; s < 16; ++s) p[s] = *(const f4*)(lds + ((s >> 1) * 16 + mp * 4 + (s & 1) * 4 + n4) * KB + v16);
            __builtin_amdgcn_sched_barrier(0);
            f4 a0 = p[0], a1 = p[1];
#pragma unroll
            for (int s = 1; s < 8; ++s) { a0 += p[2 * s]; a1 += p[2 * s + 1]; }
            u4 y = resid_pack(a0, a1, r);
            STAMP(15);
            if (opq(L) == 1) {
                const rsrc_t ro = __builtin_amdgcn_make_buffer_rsrc(out, 0, 2048 * 448 * 2, 0x00020000);
                if (ok) __builtin_amdgcn_raw_buffer_store_b128(y, ro, ((mt * 16 + t) * 448 + f) * 2, 0, 0);
            }
            if (opq(L) == 0) {
                if (ok) __builtin_amdgcn_raw_buffer_store_b128(y, rs, xo, xout, SC1);
                WAITV(0);
                BAR();
                if (tid == 0) signal(cn + (96 + 2 * T + half4) * 64);
            }
            STAMP(16);
        }
    }
}

extern "C" void kernel_launch(void* const* d_in, const int* in_sizes, int n_in, void* d_out, int out_size, void* d_ws, size_t ws_size,
                              hipStream_t stream) {
    typedef const unsigned short* cp;
    pack_kernel<<<2128 / PW, PW * 64, 0, stream>>>((cp)d_in[0], (cp)d_in[1], (cp)d_in[2], (cp)d_in[4], (cp)d_in[5], (cp)d_in[6], (char*)d_ws);
    main_kernel<<<256, 512, 0, stream>>>((char*)d_ws, (char*)d_out);
}
```

```cpp
#include <hip/hip_runtime.h>
#include <hip/hip_bf16.h>

typedef __attribute__((ext_vector_type(2))) __bf16 bf2;
typedef __attribute__((ext_vector_type(8))) __bf16 bf8;
typedef __attribute__((ext_vector_type(2))) float f2;
typedef __attribute__((ext_vector_type(4))) float f4;
typedef __attribute__((ext_vector_type(2))) unsigned u2;
typedef __attribute__((ext_vector_type(4))) unsigned u4;
typedef __attribute__((address_space(3))) void* ldsp;
typedef __attribute__((address_space(1))) unsigned gu32;
typedef __amdgpu_buffer_rsrc_t rsrc_t;
#define DI __device__ __forceinline__

#define MAP 0
#define TRIM 0
constexpr int KB = 1024;
constexpr int O_WQKV = 0, O_WO = O_WQKV + 1344 * KB, O_W13 = O_WO + 448 * KB, O_W2 = O_W13 + 3136 * KB;
constexpr int O_X = O_W2 + 1792 * KB, XSZ = 1792 * KB;
constexpr int O_O = O_X + 4 * XSZ, OSZ = 2048 * KB;
constexpr int O_H = O_O + 2 * OSZ, HSZ = 7168 * KB;
constexpr int O_CNT = O_H + 2 * HSZ, CNT_WORDS = 8 * 32 * 64;
constexpr int O_DBG = O_CNT + CNT_WORDS * 4;
constexpr int WS_BYTES = O_DBG + 256 * 72 * 8;
constexpr int LDS_STAT = 128 * KB, LDS_CS = 130 * KB, LDS_BYTES = 132 * KB;
constexpr int SC1 = 16, NW0 = 12, NW1 = 18, NW2 = 24;

DI f4 mfma(u4 a, u4 b, f4 c) {
    return __builtin_amdgcn_mfma_f32_16x16x32_bf16(__builtin_bit_cast(bf8, a), __builtin_bit_cast(bf8, b), c, 0, 0, 0);
}
typedef __attribute__((ext_vector_type(16))) float f16v;
DI f16v mfma32(u4 a, u4 b, f16v c) {
    return __builtin_amdgcn_mfma_f32_32x32x16_bf16(__builtin_bit_cast(bf8, a), __builtin_bit_cast(bf8, b), c, 0, 0, 0);
}
DI unsigned pk(float a, float b) { f2 v = {a, b}; return __builtin_bit_cast(unsigned, __builtin_convertvector(v, bf2)); }
DI float lo(unsigned x) { return __builtin_bit_cast(float, x << 16); }
DI float hi(unsigned x) { return __builtin_bit_cast(float, x & 0xffff0000u); }
DI unsigned fu(float x) { return __builtin_bit_cast(unsigned, x); }
DI float uf(unsigned x) { return __builtin_bit_cast(float, x); }
DI float sum4g(float x) {
    u2 a = __builtin_amdgcn_permlane16_swap(fu(x), fu(x), false, false);
    x = uf(a.x) + uf(a.y);
    u2 b = __builtin_amdgcn_permlane32_swap(fu(x), fu(x), false, false);
    return uf(b.x) + uf(b.y);
}
#define WAITV(n) asm volatile("s_waitcnt vmcnt(" #n ")" ::: "memory")
#define BAR() do { asm volatile("s_waitcnt lgkmcnt(0)" ::: "memory"); __builtin_amdgcn_s_barrier(); asm volatile("" ::: "memory"); } while (0)
#define PSTAMP(i)
#define STAMP(i)
#define STAMPW(i, th)

#define PW 4
#define NPF 8
typedef __attribute__((address_space(1))) char gchar;
#define PAUX 0
__global__ __launch_bounds__(PW * 64) void pack_kernel(const unsigned short* x, const unsigned short* wqkv, const unsigned short* wo,
                                                   const unsigned short* w1, const unsigned short* w3, const unsigned short* w2, char* ws) {
    const int lane = threadIdx.x & 63, t = lane & 15, g = lane >> 4, q = t >> 2, c0 = 4 * (t & 3);
    if (blockIdx.x < NPF) {
        unsigned long long pc; asm volatile("s_getpc_b64 %0" : "=s"(pc));
        const gchar* cp = (const gchar*)(pc & ~255ull);
        u4 v[32 / PW];
#pragma unroll
        for (int i = 0; i < 32 / PW; ++i) v[i] = *(const __attribute__((address_space(1))) u4*)(cp + min(i * PW * KB + (int)threadIdx.x * 16, 31 * KB + 496));
#pragma unroll
        for (int i = 0; i < 32 / PW; ++i) asm volatile("" :: "v"(v[i]));
        return;
    }
    const int bx = blockIdx.x - NPF;
    int id = __builtin_amdgcn_readfirstlane(bx * PW + (threadIdx.x >> 6));
    if (bx < CNT_WORDS / (PW * 64)) ((unsigned*)(ws + O_CNT))[bx * PW * 64 + threadIdx.x] = 0;
    const rsrc_t rw = __builtin_amdgcn_make_buffer_rsrc(ws, 0, WS_BYTES, 0x00020000);
    PSTAMP(0);
    if (id >= 1680) {
        id = (id - 1680) * 4;
        u4 v[4];
#pragma unroll
        for (int i = 0; i < 4; ++i) { int m = (id + i) / 14, s = (id + i) % 14; v[i] = *(const u4*)(x + (m * 16 + t) * 448 + s * 32 + g * 8); }
#pragma unroll
        for (int i = 0; i < 4; ++i) __builtin_amdgcn_raw_buffer_store_b128(v[i], rw, lane * 16, O_X + (id + i) * KB, PAUX);
        PSTAMP(1);
        return;
    }
    __shared__ __attribute__((aligned(16))) char tr[PW * 4 * KB];
    char* tp = tr + (threadIdx.x >> 6) * 4 * KB;
    const unsigned short* src; int ld, col, row0, dst, rows = 448; bool ok = true, perm = false, perm2 = false, ones = false;
    if (id < 336) { int s = id / 24, nt = (id % 24) * 4 + q, p = nt >> 5, j = (nt & 31) >> 1, h = nt & 1;
        src = wqkv; ld = 1344; col = p * 448 + 28 * j + 16 * h + c0; ok = !(h && c0 == 12); ones = p == 1 && !ok; row0 = 32 * s;
        dst = O_WQKV + ((j * 6 + 2 * p + h) * 14 + s) * KB;
    } else if (id < 336 + 112) { id -= 336; int s = id / 7, nt = (id % 7) * 4 + q;
        src = wo; ld = 448; col = 16 * nt + c0; row0 = 28 * s; perm = true; dst = O_WO + (nt * 16 + s) * KB;
    } else if (id < 448 + 784) { id -= 448; int s = id / 56, r = id % 56, mat = r / 28, nt = (r % 28) * 4 + q;
        src = mat ? w3 : w1; ld = 1792; col = 16 * nt + c0; row0 = 32 * s; dst = O_W13 + ((nt * 2 + mat) * 14 + s) * KB;
    } else { id -= 448 + 784; int s = id >> 3, c = id & 7;
        src = w2; ld = 448; rows = 1792; perm2 = true; col = 56 * c + 16 * q + c0; ok = 16 * q + c0 < 56; row0 = 32 * s; dst = O_W2 + ((c * 56 + s) * 4 + q) * KB; }
    const rsrc_t r = __builtin_amdgcn_make_buffer_rsrc((void*)src, 0, rows * ld * 2, 0x00020000);
    u2 v[8];
#pragma unroll
    for (int j = 0; j < 8; ++j) {
        int k = perm ? (j < 4 ? 4 * g + j : 12 + 4 * g + j) : perm2 ? 16 * (g >> 1) + 4 * (g & 1) + (j < 4 ? j : j + 4) : 8 * g + j;
        v[j] = __builtin_amdgcn_raw_buffer_load_b64(r, ok && (!perm || k < 28) ? ((row0 + k) * ld + col) * 2 : 0x7ffffff0, 0, 0);
    }
#pragma unroll
    for (int c = 0; c < 4; ++c) {
        u4 o;
#pragma unroll
        for (int i = 0; i < 4; ++i) o[i] = __builtin_amdgcn_perm(v[2 * i + 1][c >> 1], v[2 * i][c >> 1], (c & 1) ? 0x07060302u : 0x05040100u);
        if (c == 0 && ones) o = u4{0x3f803f80u, 0x3f803f80u, 0x3f803f80u, 0x3f803f80u};
        *(u4*)(tp + q * KB + (g * 16 + c0 + c) * 16) = o;
    }
#pragma unroll
    for (int c = 0; c < 4; ++c)
        __builtin_amdgcn_raw_buffer_store_b128(*(const u4*)(tp + c * KB + lane * 16), rw, lane * 16, __builtin_amdgcn_readlane(dst, 4 * c), PAUX);
    PSTAMP(1);
}

DI void wait_cnt(gu32* p, unsigned want) {
#pragma unroll 1
    for (int i = 0; i < (1 << 21); ++i) {
        if (__hip_atomic_load(p, __ATOMIC_RELAXED, __HIP_MEMORY_SCOPE_AGENT) >= want) break;
        __builtin_amdgcn_s_sleep(1);
    }
}
DI void signal(gu32* p) { __hip_atomic_fetch_add(p, 1u, __ATOMIC_RELAXED, __HIP_MEMORY_SCOPE_AGENT); }
template <int N, int I0 = 0, int M> DI void loadW(u4 (&W)[M], rsrc_t rs, int v16, int base) {
#pragma unroll
    for (int i = I0; i < N; ++i) W[i] = __builtin_amdgcn_raw_buffer_load_b128(rs, v16 + (i & 3) * KB, base + (i & ~3) * KB, 0);
}
template <int N, int I0 = 0, int M> DI void touchW(u4 (&W)[M]) {
#pragma unroll
    for (int i = I0; i < N; ++i) asm volatile("" : "+v"(W[i]));
}
template <int N, int I0 = 0, int M> DI void undefW(u4 (&W)[M]) {
#pragma unroll
    for (int i = I0; i < N; ++i) asm volatile("" : "=v"(W[i]));
}
DI int opq(int x) { asm volatile("" : "+s"(x)); return x; }
DI void dma(rsrc_t rs, char* lds, int ldsoff, int v16, int goff) {
    __builtin_amdgcn_raw_ptr_buffer_load_lds(rs, (ldsp)(lds + ldsoff), 16, v16, goff, 0, SC1);
}
#define ONES u4{0x3f803f80u, 0x3f803f80u, 0x3f803f80u, 0x3f803f80u}
DI float diag(f4 d, int t) { float a = (t & 1) ? d[1] : d[0], b = (t & 1) ? d[3] : d[2]; return (t & 2) ? b : a; }
template <bool SWAP> DI void colsum(const u4 (&W)[28], f4 (&cs)[2]) {
    const u4 ones = {0x3f803f80u, 0x3f803f80u, 0x3f803f80u, 0x3f803f80u};
    cs[0] = f4{0, 0, 0, 0}; cs[1] = f4{0, 0, 0, 0};
#pragma unroll
    for (int s = 0; s < 14; ++s) {
        cs[0] = SWAP ? mfma(ones, W[s], cs[0]) : mfma(W[s], ones, cs[0]); cs[1] = SWAP ? mfma(ones, W[14 + s], cs[1]) : mfma(W[14 + s], ones, cs[1]);
    }
}
DI u4 resid_pack(f4 a0, f4 a1, u4 r) {
    float v[8];
#pragma unroll
    for (int i = 0; i < 4; ++i) { u2 s = __builtin_amdgcn_permlane16_swap(fu(a0[i]), fu(a1[i]), false, false); v[i] = uf(s.x); v[4 + i] = uf(s.y); }
    u4 o;
#pragma unroll
    for (int i = 0; i < 4; ++i) { unsigned y = pk(v[2 * i], v[2 * i + 1]); o[i] = pk(lo(r[i]) + lo(y), hi(r[i]) + hi(y)); }
    return o;
}
DI f2 ln_fin(f2 s) {
    float mean = s.x * (1.0f / 448), var = fmaxf(s.y * (1.0f / 448) - mean * mean, 0.0f);
    return f2{mean, __builtin_amdgcn_rsqf(var + 1e-5f)};
}
DI f2 ln_stat(const char* lds, int tok) { return ln_fin(*(const f2*)(lds + LDS_STAT + tok * 8)); }
#define LDW13(i0, i1) do { const int wb = opq(w13); _Pragma("unroll") for (int i = i0; i < i1; ++i) \
                W[i] = __builtin_amdgcn_raw_buffer_load_b128(rs, wv + (i & 7) * 512, wb + (i >> 3) * 4 * KB, 0); } while (0)
template <int MODE>
DI void qkv_gemm(u4 (&W)[28], u4 (&W2)[16], rsrc_t rs, int wo_off, int w13, char* lds, int v16, int m0, int t, int g, int xsrc, int T, int w) {
    constexpr int NM = MODE == 0 ? 4 : 3;
#define XDMA(n) do { int i = min(w + 8 * (n), 125), s_ = i / 9, m = i - 9 * s_, mt = max(8 * T - 1 + m, 0); \
        dma(rs, lds, i * KB, v16, xsrc + (mt * 14 + s_) * KB); } while (0)
#pragma unroll
    for (int n = 0; n < 5; ++n) XDMA(n);
#pragma unroll
    for (int i = 0; i < 28; ++i) W[(i >> 1) + (i & 1) * 14] = *(const u4*)(lds + (44 + 28 * MODE + (i >> 1) + (i & 1) * 14) * KB + v16);
    __builtin_amdgcn_sched_barrier(0);
    f4 cs[2]; colsum<MODE == 2>(W, cs);
    const int vo = TRIM && MODE == 2 && w == 7 ? 0x7ff00000 : v16;
    f4 acc[NM][2] = {}, sq[2] = {};
    const char* ap = lds + m0 * KB + v16;
    u4 b[2][NM];
#pragma unroll
    for (int s = 0; s < 14; ++s) {
        const bool first = s == 0 || s == 3 || s == 7 || s == 10, last = s == 2 || s == 6 || s == 9 || s == 13;
        if (s == 0) { WAITV(1); BAR(); } else if (s == 3) { WAITV(6); BAR(); } else if (s == 7) { WAITV(10); BAR(); } else if (s == 10) { WAITV(12); BAR(); }
#pragma unroll
        for (int mi = 0; mi < NM; ++mi) {
            if (first) b[s & 1][mi] = *(const u4*)(ap + (s * 9 + mi) * KB);
        }
#pragma unroll
        for (int mi = 0; mi < NM; ++mi) {
            if (!last) b[(s & 1) ^ 1][mi] = *(const u4*)(ap + (s * 9 + 9 + mi) * KB);
            u4 x = b[s & 1][mi];
            acc[mi][0] = MODE == 2 ? mfma(x, W[s], acc[mi][0]) : mfma(W[s], x, acc[mi][0]);
            acc[mi][1] = MODE == 2 ? mfma(x, W[14 + s], acc[mi][1]) : mfma(W[14 + s], x, acc[mi][1]);
            if (MODE == 1 && mi < 2) sq[mi] = mfma(x, x, sq[mi]);
            if (MODE == 2 && mi == 2) sq[0] = mfma(x, x, sq[0]);
            if (mi < 3 && 5 + 3 * s + mi < 16) { XDMA(5 + 3 * s + mi); __builtin_amdgcn_sched_barrier(0); }
        }
        if (s >= 4 && s < 12) {
#pragma unroll
            for (int i = 2 * (s - 4); i < 2 * (s - 3); ++i) W2[i] = __builtin_amdgcn_raw_buffer_load_b128(rs, vo + (i & 3) * KB, wo_off + (i & ~3) * KB, 0);
        }
        __builtin_amdgcn_sched_barrier(0);
    }
    float* sp = (float*)(lds + LDS_STAT) + (m0 * 16 + t) * 2;
    if (MODE == 1) {
        if (g == 3) { sp[0] = acc[0][1][0]; sp[32] = acc[1][1][0]; sp[64] = acc[2][1][0]; }
        if (g == (t >> 2)) { sp[1] = diag(sq[0], t); sp[33] = diag(sq[1], t); }
    }
    if (MODE == 2) { if (g == (t >> 2)) sp[65] = diag(sq[0], t); }
    BAR();
    const int wv = TRIM && MODE == 2 && w == 7 ? 0x7ff00000 : (g & 1) * 14 * KB + (g >> 1) * 256 + t * 16;
    LDW13(0, NW0);
#pragma unroll
    for (int mi = 0; mi < NM; ++mi) {
        f4 y0, y1;
        if (MODE < 2) {
            f2 st = ln_stat(lds, (m0 + mi) * 16 + t);
            y0 = (acc[mi][0] - cs[0] * st.x) * st.y; y1 = (acc[mi][1] - cs[1] * st.x) * st.y;
            if (g == 3) y1 = f4{0, 0, 0, 0};
        } else {
            const f4* p = (const f4*)(lds + LDS_STAT + ((m0 + mi) * 16 + 4 * g) * 8);
            f4 s01 = p[0], s23 = p[1];
            f2 st[4] = {ln_fin(f2{s01[0], s01[1]}), ln_fin(f2{s01[2], s01[3]}), ln_fin(f2{s23[0], s23[1]}), ln_fin(f2{s23[2], s23[3]})};
#pragma unroll
            for (int r = 0; r < 4; ++r) { y0[r] = (acc[mi][0][r] - cs[0][r] * st[r].x) * st[r].y; y1[r] = (acc[mi][1][r] - cs[1][r] * st[r].x) * st[r].y; }
        }
        *(u4*)(lds + (MODE * 9 + m0 + mi) * KB + v16) = u4{pk(y0[0], y0[1]), pk(y0[2], y0[3]), pk(y1[0], y1[1]), pk(y1[2], y1[3])};
    }
    BAR();
    touchW<16>(W2); touchW<NW0>(W);
}
DI float max4g(float x) {
    u2 a = __builtin_amdgcn_permlane16_swap(fu(x), fu(x), false, false);
    x = fmaxf(uf(a.x), uf(a.y));
    u2 b = __builtin_amdgcn_permlane32_swap(fu(x), fu(x), false, false);
    return fmaxf(uf(b.x), uf(b.y));
}

__global__ __launch_bounds__(512, 2) void main_kernel(char* ws, char* out) {
    __shared__ __attribute__((aligned(1024))) char lds[LDS_BYTES];
    const rsrc_t rs = __builtin_amdgcn_make_buffer_rsrc(ws, 0, WS_BYTES, 0x00020000);
    gu32* cnt = (gu32*)(ws + O_CNT);
    const int w_ = __builtin_amdgcn_readfirstlane(threadIdx.x >> 6);
#define IDX() int lane, w = w_, bid = blockIdx.x; asm volatile("v_mbcnt_lo_u32_b32 %0, -1, 0\n\tv_mbcnt_hi_u32_b32 %0, -1, %0" : "=v"(lane)); \
    asm volatile("" : "+s"(w), "+s"(bid)); \
    const int tid = w * 64 + lane, t = lane & 15, g = lane >> 4, v16 = lane * 16, xc = bid & 7, sl = bid >> 3, w7 = w < 7 ? w : 6; \
    const int T = MAP ? 2 * xc + (sl >> 4) : 4 * (xc >> 1) + (sl >> 3), j = MAP ? sl & 15 : 8 * (xc & 1) + (sl & 7);     \
    const int c2 = MAP ? sl & 3 : 2 * (xc & 1) + (sl & 1), sub2 = MAP ? (sl >> 2) & 3 : (sl >> 1) & 3;             \
    const int c4 = MAP ? sl & 7 : 4 * (xc & 1) + (sl & 3), half4 = MAP ? (sl >> 3) & 1 : (sl >> 2) & 1;            \
    const int p1 = w < 2 ? 0 : w < 5 ? 1 : 2, wq_off = O_WQKV + (j * 6 + 2 * p1) * 14 * KB; \
    (void)t; (void)g; (void)w7; (void)c2; (void)sub2; (void)c4; (void)half4; (void)wq_off; (void)T
    u4 W[28], W2[16];
#pragma unroll 1
    for (int L = 0; L < 2; ++L) {
        const int xin = O_X + 2 * L * XSZ, xmid = xin + XSZ, xout = xmid + XSZ, ob = O_O + L * OSZ, hb = O_H + L * HSZ;
        gu32* cn = cnt + L * 4 * 32 * 64;
        {
            IDX();
            STAMP(0);
            const int m0 = w < 2 ? 1 + 4 * w : w < 5 ? 3 * (w - 2) : 3 * (w - 5);
#pragma unroll
            for (int n = 0; n < 11; ++n) { int i = min(w + 8 * n, 83); __builtin_amdgcn_raw_ptr_buffer_load_lds(rs, (ldsp)(lds + (44 + i) * KB), 16, v16, O_WQKV + (j * 84 + i) * KB, 0, 0); }
            if (L == 0 && w == 7 && sl < 7) {
                unsigned long long pc; asm volatile("s_getpc_b64 %0" : "=s"(pc));
                unsigned pf = *(const gu32*)((const gchar*)(pc & ~63ull) + sl * 4 * KB + lane * 64);
                asm volatile("" :: "v"(pf));
            }
            if (L > 0 && w == 0) {
                int i = 2 * T - 1 + lane;
                if (lane < 3 && i >= 0) wait_cnt(cn - 32 * 64 + i * 64, 8);
            }
            WAITV(0);
            BAR();
            STAMP(1);
            const int wo_off = O_WO + (7 * c2 + w7) * 16 * KB, w13 = O_W13 + (7 * j + w7) * 28 * KB;
            undefW<16>(W2); undefW<NW0>(W);
            if (opq(p1) == 0) { qkv_gemm<0>(W, W2, rs, wo_off, w13, lds, v16, m0, t, g, xin, T, w); }
            if (opq(p1) == 1) { qkv_gemm<1>(W, W2, rs, wo_off, w13, lds, v16, m0, t, g, xin, T, w); }
            if (opq(p1) == 2) qkv_gemm<2>(W, W2, rs, wo_off, w13, lds, v16, m0, t, g, xin, T, w);
            STAMP(2);
            const char* qp = lds + (w + 1) * KB + v16;
            const u4 q = *(const u4*)qp, v0 = *(const u4*)(qp + 17 * KB), v1 = *(const u4*)(qp + 18 * KB);
            f4 lg[2] = {mfma(*(const u4*)(qp + 8 * KB), q, f4{0, 0, 0, 0}), mfma(*(const u4*)(qp + 9 * KB), q, f4{0, 0, 0, 0})};
            const int kmin = (T == 0 && w == 0) ? 16 : 0;
            float mx = -3.0e38f;
#pragma unroll
            for (int i = 0; i < 8; ++i) {
                int key = 16 * (i >> 2) + 4 * g + (i & 3), d = 16 + t - key;
                bool valid = d >= 0 && d <= 14 && !(d & 1) && key >= kmin;
                lg[i >> 2][i & 3] = valid ? lg[i >> 2][i & 3] * (0.18898223650461361f * 1.4426950408889634f) : -3.0e38f;
                mx = fmaxf(mx, lg[i >> 2][i & 3]);
            }
            mx = max4g(mx);
            float p[8], den = 0;
#pragma unroll
            for (int i = 0; i < 8; ++i) { p[i] = __builtin_amdgcn_exp2f(lg[i >> 2][i & 3] - mx); den += p[i]; }
            const float inv = __builtin_amdgcn_rcpf(sum4g(den));
            const u4 pb = {pk(p[0] * inv, p[1] * inv), pk(p[2] * inv, p[3] * inv), pk(p[4] * inv, p[5] * inv), pk(p[6] * inv, p[7] * inv)};
            const f4 o0 = mfma(u4{v0.x, v0.y, v1.x, v1.y}, pb, f4{0, 0, 0, 0}), o1 = mfma(u4{v0.z, v0.w, v1.z, v1.w}, pb, f4{0, 0, 0, 0});
            __builtin_amdgcn_raw_buffer_store_b128(u4{pk(o0[0], o0[1]), pk(o0[2], o0[3]), pk(o1[0], o1[1]), pk(o1[2], o1[3])}, rs, v16,
                                                   ob + ((8 * T + w) * 16 + j) * KB, SC1);
            STAMP(3);
            WAITV(0);
            BAR();
            if (tid == 448) signal(cn + T * 64);
        }
        {
            IDX();
            STAMP(4);
            const int mt0 = 8 * T + 2 * sub2;
            const int f = 112 * c2 + 16 * w7 + 8 * (g >> 1);
            const int xo = ((mt0 + (g & 1)) * 14 + (f >> 5)) * KB + (((f & 31) >> 3) * 16 + t) * 16;
            const int wv = (g & 1) * 14 * KB + (g >> 1) * 256 + t * 16, w13 = O_W13 + (7 * j + w7) * 28 * KB;
            u4 r = {};
            f4 csum = {};
            const unsigned on = (t == (g & 1)) ? 0x3f803f80u : 0u;
            undefW<28, NW0>(W);
            if (opq(w) == 7) { if (lane == 0) wait_cnt(cn + T * 64, 16); }
            if (opq(w) != 7) {
                r = __builtin_amdgcn_raw_buffer_load_b128(rs, xo, xin, SC1);
                LDW13(NW0, NW1);
            }
#pragma unroll
            for (int i = 0; i < NW0; ++i) csum = mfma(W[i], u4{on, on, on, on}, csum);
            STAMP(5);
            touchW<NW1, NW0>(W);
            asm volatile("" : "+v"(r));
            BAR();
#pragma unroll
            for (int n = 0; n < 4; ++n) dma(rs, lds, (w * 4 + n) * KB, v16, ob + (mt0 * 16 + w * 4 + n) * KB);
#pragma unroll
            for (int i = NW0; i < NW1; ++i) csum = mfma(W[i], u4{on, on, on, on}, csum);
            WAITV(0);
            BAR();
            STAMP(6);
            if (opq(w) != 7) {
                f4 a0 = {}, a1 = {};
                u4 b[12];
#pragma unroll
                for (int s = 0; s < 10; ++s) b[s] = *(const u4*)(lds + ((s >> 1) + (s & 1) * 16) * KB + v16);
#pragma unroll
                for (int s = 0; s < 16; ++s) {
                    if (s < 11) { b[(2 * s + 10) % 12] = *(const u4*)(lds + (s + 5) * KB + v16); b[(2 * s + 11) % 12] = *(const u4*)(lds + (s + 21) * KB + v16); }
                    a0 = mfma(W2[s], b[(2 * s) % 12], a0); a1 = mfma(W2[s], b[(2 * s + 1) % 12], a1);
                    if (NW1 + s < NW2) LDW13(NW1 + s, NW1 + s + 1);
                    __builtin_amdgcn_sched_barrier(0);
                }
                __builtin_amdgcn_raw_buffer_store_b128(resid_pack(a0, a1, r), rs, xo, xmid, SC1);
            }
            STAMP(7);
            WAITV(0);
            BAR();
            if (tid == 448) signal(cn + (32 + T) * 64);
            STAMP(8);
            if (opq(w) == 7) { if (lane == 0) wait_cnt(cn + (32 + T) * 64, 16); }
            if (opq(w) != 7) {
                LDW13(NW2, 28);
            }
#pragma unroll
            for (int i = NW1; i < NW2; ++i) csum = mfma(W[i], u4{on, on, on, on}, csum);
            touchW<28, NW2>(W);
            BAR();
            STAMP(9);
#pragma unroll
            for (int n = 0; n < 4; ++n) dma(rs, lds, (w + 8 * n) * KB, v16, xmid + (8 * T * 14 + w + 8 * n) * KB);
#pragma unroll
            for (int i = NW2; i < 28; ++i) csum = mfma(W[i], u4{on, on, on, on}, csum);
            if (t < 2) *(f4*)(lds + LDS_CS + w * 128 + t * 64 + g * 16) = csum;
        }
        {
            IDX();
            const int wv3 = (g & 1) * 14 * KB + (g >> 1) * 256 + t * 16;
            const int hk = lane >> 5, f = 112 * j + 16 * w7;
            const int ho = ((8 * T + (g & 1)) * 56 + (f >> 5)) * KB + ((((f & 31) >> 3) + hk) * 16 + t) * 16;
            const int w4 = O_W2 + (c4 * 56 + 7 * w) * 4 * KB, xs = xmid + (8 * T * 14 + w) * KB;
            const int v16p = TRIM && t >= 8 ? 0x7ff00000 : v16;
#define LDW4(i) __builtin_amdgcn_raw_buffer_load_b128(rs, (((i) & 3) == 3 ? v16p : v16) + ((i) & 3) * KB, w4 + ((i) & ~3) * KB, 0)
#define P3WAIT(c) do { if (c == 0) WAITV(0); else if (c == 1) WAITV(7); else if (c == 2) WAITV(11); else if (c == 3) WAITV(16); BAR(); } while (0)
            undefW<16>(W2);
            if (opq(w) == 7) {
#pragma unroll
                for (int c = 0; c < 5; ++c) {
                    P3WAIT(c);
                    if (c < 4) {
                        f4 s1[2] = {}, s2[2] = {};
                        u4 b[7];
#define HB(s) *(const u4*)(lds + ((2 * c + ((s) & 1)) * 14 + ((s) >> 1)) * KB + v16)
#pragma unroll
                        for (int s = 0; s < 6; ++s) b[s] = HB(s);
#pragma unroll
                        for (int s = 0; s < 28; ++s) {
                            if (s < 22) b[(s + 6) % 7] = HB(s + 6);
                            s1[s & 1] = mfma(ONES, b[s % 7], s1[s & 1]); s2[s & 1] = mfma(b[s % 7], b[s % 7], s2[s & 1]);
                            if (c == 0 && s < 20 && !(s & 1)) dma(rs, lds, (w + 32 + 4 * s) * KB, v16, xs + (32 + 4 * s) * KB);
                            if ((c == 1 || c == 2) && s % 3 == 1 && s < 24) W2[8 * (c - 1) + s / 3] = LDW4(8 * (c - 1) + s / 3);
                            if (c == 3 && s >= 2 && s <= 24 && !(s & 1)) W[s / 2 - 1] = LDW4(15 + s / 2);
                            __builtin_amdgcn_sched_barrier(0);
                        }
#pragma unroll
                        for (int mi = 0; mi < 2; ++mi)
                            if (g == (t >> 2)) *(f2*)(lds + LDS_STAT + ((2 * c + mi) * 16 + t) * 8) = ln_fin(f2{s1[mi][0], diag(s2[mi], t)});
                    }
                }
                touchW<16>(W2); touchW<12>(W); undefW<28, 12>(W);
            }
            if (opq(w) != 7) {
                f16v a[2];
                f4 cs[4];
#pragma unroll
                for (int c = 0; c < 5; ++c) {
                    P3WAIT(c);
                    const char* ap = lds + 2 * c * 14 * KB + wv3;
                    u4 b[3];
                    f2 st;
                    float r1, r2;
                    unsigned hp[4];
                    if (c == 0) {
#pragma unroll
                        for (int i = 0; i < 4; ++i) cs[i] = *(const f4*)(lds + LDS_CS + w * 128 + i * 32 + hk * 16);
                    }
                    if (c < 4) { b[0] = *(const u4*)ap; b[1] = *(const u4*)(ap + 512); }
                    if (c > 0) { st = *(const f2*)(lds + LDS_STAT + ((2 * c - 2) * 16 + (lane & 31)) * 8); r1 = st.y * -1.4426950408889634f; r2 = st.y * st.y; }
                    float h0;
#pragma unroll
                    for (int s = 0; s < 28; ++s) {
                        if (c < 4) {
                            if (s < 26) b[(s + 2) % 3] = *(const u4*)(ap + (s + 2) * 512);
                            a[c & 1] = mfma32(W[s], b[s % 3], s ? a[c & 1] : f16v{});
                        }
                        if (c == 0 && s < 20 && !(s & 1)) dma(rs, lds, (w + 32 + 4 * s) * KB, v16, xs + (32 + 4 * s) * KB);
                        if (c > 0 && s >= 2 && s < 18 && !(s & 1)) {
                            const int i = (s - 2) >> 1;
                            float ga = __builtin_fmaf(-cs[i >> 2][i & 3], st.x, a[(c & 1) ^ 1][i]);
                            asm volatile("" : "+v"(ga));
                            float up = __builtin_fmaf(-cs[2 + (i >> 2)][i & 3], st.x, a[(c & 1) ^ 1][8 + i]);
                            float gu = ga * up;
                            asm volatile("" : "+v"(gu));
                            float h = gu * (r2 * __builtin_amdgcn_rcpf(1.0f + __builtin_amdgcn_exp2f(ga * r1)));
                            if (i & 1) { hp[i >> 1] = pk(h0, h); asm volatile("" : "+v"(hp[i >> 1])); } else { h0 = h; asm volatile("" : "+v"(h0)); }
                        }
                        if (c > 0 && s == 18)
                            __builtin_amdgcn_raw_buffer_store_b128(u4{hp[0], hp[1], hp[2], hp[3]}, rs, ho, hb + 2 * (c - 1) * 56 * KB, SC1);
                        if ((c == 1 || c == 2) && s % 3 == 1 && s < 24) W2[8 * (c - 1) + s / 3] = LDW4(8 * (c - 1) + s / 3);
                        if (c == 3 && s >= 2 && s <= 24 && !(s & 1)) W[s / 2 - 1] = LDW4(15 + s / 2);
                        if (c < 4) __builtin_amdgcn_sched_barrier(0);
                    }
                    if (c == 3) STAMP(10);
                }
            }
            STAMP(11);
            WAITV(0);
            touchW<16>(W2); touchW<12>(W);
            BAR();
            if (tid == 0) signal(cn + (64 + T) * 64);
        }
        {
            IDX();
            STAMP(12);
            const int mt0 = 8 * T + 4 * half4;
            const int n4 = w & 3, mp = 2 * (w >> 2), f = 56 * c4 + 16 * n4 + 8 * (g >> 1), mt = mt0 + mp + (g & 1);
            const bool ok = n4 < 3 || g < 2;
            const int xo = ok ? (mt * 14 + (f >> 5)) * KB + (((f & 31) >> 3) * 16 + t) * 16 : 0;
            u4 r = __builtin_amdgcn_raw_buffer_load_b128(rs, xo, xmid, SC1);
            if (tid == 0) wait_cnt(cn + (64 + T) * 64, 16);
            BAR();
            STAMP(13);
            const int hsrc = hb + (mt0 * 56 + 7 * w) * KB;
            constexpr int NA = 18;
            u4 A[NA];
#define LDA(i) __builtin_amdgcn_raw_buffer_load_b128(rs, v16 + (((i) % 7) & 3) * KB, hsrc + (((i) / 7) * 56 + (((i) % 7) & 4)) * KB, SC1)
#pragma unroll
            for (int i = 0; i < NA; ++i) A[i] = LDA(i);
            f4 acc[2][4];
#pragma unroll
            for (int i = 0; i < 28; ++i) {
                const int m = i / 7, s = i % 7;
#pragma unroll
                for (int n = 0; n < 4; ++n) {
                    const int k = s * 4 + n;
                    acc[m & 1][n] = mfma(k < 16 ? W2[k & 15] : W[k < 16 ? 0 : k - 16], A[i % NA], s ? acc[m & 1][n] : f4{0, 0, 0, 0});
                }
                if (i + NA < 28) A[i % NA] = LDA(i + NA);
                if (m > 0 && s >= 1 && s <= 4) *(f4*)(lds + (w * 16 + (m - 1) * 4 + s - 1) * KB + v16) = acc[(m & 1) ^ 1][s - 1];
                __builtin_amdgcn_sched_barrier(0);
            }
            STAMP(14);
#pragma unroll
            for (int n = 0; n < 4; ++n) *(f4*)(lds + (w * 16 + 12 + n) * KB + v16) = acc[1][n];
            BAR();
            f4 p[16];
#pragma unroll
            for (int s = 0; s < 16; ++s) p[s] = *(const f4*)(lds + ((s >> 1) * 16 + mp * 4 + (s & 1) * 4 + n4) * KB + v16);
            __builtin_amdgcn_sched_barrier(0);
            f4 a0 = p[0], a1 = p[1];
#pragma unroll
            for (int s = 1; s < 8; ++s) { a0 += p[2 * s]; a1 += p[2 * s + 1]; }
            u4 y = resid_pack(a0, a1, r);
            STAMP(15);
            if (opq(L) == 1) {
                const rsrc_t ro = __builtin_amdgcn_make_buffer_rsrc(out, 0, 2048 * 448 * 2, 0x00020000);
                if (ok) __builtin_amdgcn_raw_buffer_store_b128(y, ro, ((mt * 16 + t) * 448 + f) * 2, 0, 0);
            }
            if (opq(L) == 0) {
                if (ok) __builtin_amdgcn_raw_buffer_store_b128(y, rs, xo, xout, SC1);
                WAITV(0);
                BAR();
                if (tid == 0) signal(cn + (96 + 2 * T + half4) * 64);
            }
            STAMP(16);
        }
    }
}

extern "C" void kernel_launch(void* const* d_in, const int* in_sizes, int n_in, void* d_out, int out_size, void* d_ws, size_t ws_size,
                              hipStream_t stream) {
    typedef const unsigned short* cp;
    pack_kernel<<<2128 / PW + NPF, PW * 64, 0, stream>>>((cp)d_in[0], (cp)d_in[1], (cp)d_in[2], (cp)d_in[4], (cp)d_in[5], (cp)d_in[6], (char*)d_ws);
    main_kernel<<<256, 512, 0, stream>>>((char*)d_ws, (char*)d_out);
}
```
